```python
import torch

def _softmax(x, axis=-1):
    return torch.softmax(x, dim=axis)
def _gelu(x):
    return torch.nn.functional.gelu(x, approximate="tanh")
def _silu(x):
    return torch.nn.functional.silu(x)
def _sigmoid(x):
    return torch.sigmoid(x)
def _layernorm(x, axis=-1, eps=1e-5):
    m = x.mean(dim=axis, keepdim=True)
    v = x.var(dim=axis, keepdim=True, unbiased=False)
    return (x - m) / torch.sqrt(v + eps)
def _scatter_add(tgt, idx, src, axis=0):
    return tgt.clone().index_add_(axis, idx, src)
def _segment_sum(src, seg, n):
    out = torch.zeros((n,) + src.shape[1:], dtype=src.dtype)
    return out.index_add_(0, seg, src)
def _pad(x, pw):
    flat = tuple(v for pair in reversed(pw) for v in pair)
    return torch.nn.functional.pad(x, flat)

kernel_name = 'recurrent_scan__compressed_kv_cla__dense_activation_mish__prompt'

def setup_inputs():

    _w = (lambda a: (lambda t: t.to(torch.bfloat16) if t.is_floating_point() else t)(torch.as_tensor(a)))
    def _A_setup():
        B, H, Sq, Sk, D, M = 1, 16, 2048, 2048, 256, 448
        Dc = 4 * D
        return {
            "x": _w((lambda *s: torch.randn(*s, dtype=torch.bfloat16) * 0.05)(B, Sq, M)),
            "c_kv": _w((lambda *s: torch.randn(*s, dtype=torch.bfloat16) * 0.8)(B, Sk, Dc)),
            "w_q": _w((lambda *s: torch.randn(*s, dtype=torch.bfloat16) * 1.6)(M, H, D) * (M ** -0.5)),
            "w_uk": _w((lambda *s: torch.randn(*s, dtype=torch.bfloat16) * 1.6)(Dc, H, D) * (Dc ** -0.5)),
            "w_uv": _w((lambda *s: torch.randn(*s, dtype=torch.bfloat16) * 0.8)(Dc, H, D) * (Dc ** -0.5)),
            "w_o": _w((lambda *s: torch.randn(*s, dtype=torch.bfloat16) * 0.8)(H, D, M) * ((H * D) ** -0.5)),
        }
    _A_in = _A_setup()

    def _B_setup():
        B, S, D, F = 1, 2048, 448, 1792
        return {
            "x": _w((lambda *s: torch.randn(*s, dtype=torch.bfloat16) * 0.05)(B, S, D)),
            "w1": _w((lambda *s: torch.randn(*s, dtype=torch.bfloat16) * 0.8)(D, F) * (D ** -0.5)),
            "b1": _w((lambda *s: torch.randn(*s, dtype=torch.bfloat16) * 0.8)(F)),
            "w2": _w((lambda *s: torch.randn(*s, dtype=torch.bfloat16) * 0.2)(F, D) * (F ** -0.5)),
        }
    _B_in = _B_setup()

    return {
        "A_x": _A_in["x"],
        "A_c_kv": _A_in["c_kv"],
        "A_w_q": _A_in["w_q"],
        "A_w_uk": _A_in["w_uk"],
        "A_w_uv": _A_in["w_uv"],
        "A_w_o": _A_in["w_o"],
        "B_x": _B_in["x"],
        "B_w1": _B_in["w1"],
        "B_b1": _B_in["b1"],
        "B_w2": _B_in["w2"],
    }

def reference(A_x, A_c_kv, A_w_q, A_w_uk, A_w_uv, A_w_o, B_x, B_w1, B_b1, B_w2):
    def _A_ref(x, c_kv, w_q, w_uk, w_uv, w_o):
        B, Sq, M = x.shape[0], x.shape[1], x.shape[2]
        Sk = c_kv.shape[1]
        H, D = w_q.shape[1], w_q.shape[2]
        q = torch.einsum("bqm,mhd->bhqd", x, w_q)
        k = torch.einsum("bkc,chd->bhkd", c_kv, w_uk)
        v = torch.einsum("bkc,chd->bhkd", c_kv, w_uv)
        scores = torch.einsum("bhqd,bhkd->bhqk", q, k) * (D ** -0.5)
        off = Sk - Sq
        qi = torch.arange(Sq)[:, None]
        kj = torch.arange(Sk)[None, :]
        cmask = kj <= (qi + off)
        scores = torch.where(cmask[None, None], scores, -1e9)
        p = _softmax(scores, axis=-1)
        ctx = torch.einsum("bhqk,bhkd->bhqd", p, v)
        return torch.einsum("bhqd,hdm->bqm", ctx, w_o)

    def _B_ref(x, w1, b1, w2):
        h = torch.einsum("bsd,df->bsf", x, w1) + b1
        sp = torch.log1p(torch.exp(-torch.abs(h))) + torch.where(h > 0, h, torch.zeros_like(h))
        act = h * torch.tanh(sp)
        return torch.einsum("bsf,fd->bsd", act, w2)

    x = A_x
    xn = _layernorm(x, axis=-1)
    s = _A_ref(xn, A_c_kv, A_w_q, A_w_uk, A_w_uv, A_w_o)
    g = _silu(_B_ref(xn, B_w1, B_b1, B_w2))
    return x + s * g
```

```cpp
#include <hip/hip_runtime.h>
#include <hip/hip_bf16.h>
#include <stdint.h>

typedef short bf16x8 __attribute__((ext_vector_type(8)));
typedef short s16x4 __attribute__((ext_vector_type(4)));
typedef float f32x4 __attribute__((ext_vector_type(4)));
typedef float f32x16 __attribute__((ext_vector_type(16)));
typedef unsigned u32x2 __attribute__((ext_vector_type(2)));
typedef unsigned u32x4 __attribute__((ext_vector_type(4)));
typedef unsigned short u16;
#define LDS_AS __attribute__((address_space(3)))

constexpr int SQ = 2048, MD = 448, MDP = 512, DC = 1024, NH = 16, HD = 256, HDALL = NH * HD, FF = 1792;

constexpr size_t WS_XN = 0;
constexpr size_t WS_Q = WS_XN + (size_t)SQ * MDP * 2;
constexpr size_t WS_K = WS_Q + (size_t)NH * SQ * HD * 2;
constexpr size_t WS_VT = WS_K + (size_t)NH * SQ * HD * 2;
constexpr size_t WS_ACT = WS_VT + (size_t)NH * SQ * HD * 2;
constexpr size_t WS_CTX = WS_ACT + (size_t)SQ * FF * 2;
constexpr size_t WS_END = WS_CTX + (size_t)SQ * HDALL * 2;

__device__ __forceinline__ float bf2f(u16 v) { return __uint_as_float(((unsigned)v) << 16); }
__device__ __forceinline__ u16 f2bf(float f) { __hip_bfloat16 b = __float2bfloat16(f); return *reinterpret_cast<u16*>(&b); }
__device__ __forceinline__ float bfr(float f) { return bf2f(f2bf(f)); }
__device__ __forceinline__ unsigned pk2(float lo, float hi) {
  unsigned r; asm("v_cvt_pk_bf16_f32 %0, %1, %2" : "=v"(r) : "v"(lo), "v"(hi)); return r;
}


__device__ __forceinline__ float mish_ref(float h) {
  const float L2E = 1.4426950408889634f, LN2 = 0.6931471805599453f;
  float e = bfr(__builtin_amdgcn_exp2f(-fabsf(h) * L2E));
  float u = 1.0f + e;
  float lu = __builtin_amdgcn_logf(u) * LN2;
  float l = bfr(u == 1.0f ? e : lu * (e * __builtin_amdgcn_rcpf(u - 1.0f)));
  float sp = bfr(l + fmaxf(h, 0.f));
  float y = fminf(2.0f * sp, 80.0f);
  float w = __builtin_amdgcn_exp2f(y * L2E);
  float lw = __builtin_amdgcn_logf(w) * LN2;
  float em = (w == 1.0f) ? y : (w - 1.0f) * (y * __builtin_amdgcn_rcpf(lw));
  float th = bfr(em * __builtin_amdgcn_rcpf(em + 2.0f));
  return h * th;
}

__global__ __launch_bounds__(256) void ln_kernel(const u16* __restrict__ x, u16* __restrict__ xn) {
  const int row = blockIdx.x * 4 + (threadIdx.x >> 6), lane = threadIdx.x & 63;
  float v[8];
  const bool act = lane < 56;
  if (act) {
    bf16x8 r = *reinterpret_cast<const bf16x8*>(x + (size_t)row * MD + lane * 8);
#pragma unroll
    for (int j = 0; j < 8; ++j) v[j] = bf2f((u16)r[j]);
  } else {
#pragma unroll
    for (int j = 0; j < 8; ++j) v[j] = 0.f;
  }
  float s = 0.f;
#pragma unroll
  for (int j = 0; j < 8; ++j) s += v[j];
#pragma unroll
  for (int o = 32; o >= 1; o >>= 1) s += __shfl_xor(s, o);
  const float mean = s * (1.0f / MD);
  float q = 0.f;
  if (act) {
#pragma unroll
    for (int j = 0; j < 8; ++j) { float d = v[j] - mean; q += d * d; }
  }
#pragma unroll
  for (int o = 32; o >= 1; o >>= 1) q += __shfl_xor(q, o);
  const float mb = bfr(mean), vb = bfr(q * (1.0f / MD));
  const float den = bfr(sqrtf(bfr(vb + 1e-5f)));
  bf16x8 o8;
#pragma unroll
  for (int j = 0; j < 8; ++j) o8[j] = act ? (short)f2bf(bfr(v[j] - mb) / den) : (short)0;
  *reinterpret_cast<bf16x8*>(xn + (size_t)row * MDP + lane * 8) = o8;
}

constexpr int BM = 256, BK = 64, HALF = 128, HT = HALF * BK, GEMM_SHM = 8 * HT * 2;

__device__ __forceinline__ int lds_byte(int r, int c) {
  int st = (r >> 4) * 2 + (c >> 5), rr = r & 15, cc = c & 31, ob = rr * 64 + cc * 2;
  return st * 1024 + (ob ^ (((ob >> 9) & 1) << 5));
}
__device__ __forceinline__ void stage_rc(int b, int& R, int& C) {
  int st = b / 1024, sb = b % 1024, swz = sb ^ (((sb >> 9) & 1) << 5);
  R = (st >> 1) * 16 + swz / 64; C = (st & 1) * 32 + (swz % 64) / 2;
}

enum { EPI_ROWMAJOR = 0, EPI_VT = 1, EPI_MISH = 2 };

template <int EPI>
__device__ __forceinline__ void gemm_tile(const u16* __restrict__ A, int lda, const u16* __restrict__ Bg, int ldb, int kmax, int nt,
                                          int brow, int bcol, u16* __restrict__ out, int ldo, const u16* __restrict__ bias, char* shm_raw) {
  u16* shm = (u16*)shm_raw;
#define SA(b, h) (shm + ((b) * 2 + (h)) * HT)
#define SB(b, h) (shm + (4 + (b) * 2 + (h)) * HT)
#define STAGE_A(T, P, br, kt) do { const u16* _g = A + (long)(br) * lda + (long)(kt) * BK;                     \
    for (int _i = 0; _i < 2; ++_i) { int _b = (T) * 16 + _i * 8192; int _r, _c; stage_rc(_b, _r, _c);          \
      __builtin_amdgcn_global_load_lds((const unsigned*)(_g + (long)_r * lda + _c), (LDS_AS unsigned*)((char*)(P) + _b), 16, 0, 0); } } while (0)
#define STAGE_B(T, P, bc, kt) do {                                                                              \
    for (int _i = 0; _i < 2; ++_i) { int _b = (T) * 16 + _i * 8192; int _k = _b >> 8, _pc = (_b >> 4) & 15;     \
      int _c = _pc ^ (((_k & 3) | (((_k >> 3) & 1) << 2)) << 1); int _kr = (kt) * BK + _k; _kr = _kr < kmax ? _kr : kmax - 1; \
      __builtin_amdgcn_global_load_lds((const unsigned*)(Bg + (long)_kr * ldb + (bc) + _c * 8), (LDS_AS unsigned*)((char*)(P) + _b), 16, 0, 0); } } while (0)
#define LDA(dst, b, h) for (int m = 0; m < 4; ++m) for (int k = 0; k < 2; ++k) \
    dst[m][k] = *reinterpret_cast<const bf16x8*>((char*)SA(b, h) + lds_byte(wr * 64 + m * 16 + fr, k * 32 + fq * 8))
#define LDB(dst, b, h) for (int n = 0; n < 2; ++n) for (int k = 0; k < 2; ++k) {                                \
    LDS_AS char* _p = (LDS_AS char*)SB(b, h) + bbase + n * 32 + k * (32 * 256);                                 \
    _p = (LDS_AS char*)((unsigned)(uintptr_t)(LDS_AS char*)SB(b, h) + ((bbase + n * 32) ^ bxor) + k * (32 * 256)); \
    s16x4 _lo = __builtin_amdgcn_ds_read_tr16_b64_v4i16((LDS_AS s16x4*)_p);                                     \
    s16x4 _hi = __builtin_amdgcn_ds_read_tr16_b64_v4i16((LDS_AS s16x4*)(_p + 1024));                            \
    dst[n][k] = (bf16x8){_lo[0], _lo[1], _lo[2], _lo[3], _hi[0], _hi[1], _hi[2], _hi[3]}; }
#define MMA(ai, bj, At, Bt) do { __builtin_amdgcn_s_setprio(1);                                                 \
    for (int m = 0; m < 4; ++m) for (int n = 0; n < 2; ++n) for (int k = 0; k < 2; ++k) {                         \
      if constexpr (EPI == EPI_VT) acc[ai][bj][m][n] = __builtin_amdgcn_mfma_f32_16x16x32_bf16(At[m][k], Bt[n][k], acc[ai][bj][m][n], 0, 0, 0); \
      else acc[ai][bj][m][n] = __builtin_amdgcn_mfma_f32_16x16x32_bf16(Bt[n][k], At[m][k], acc[ai][bj][m][n], 0, 0, 0); }                     \
    __builtin_amdgcn_s_setprio(0); } while (0)
#define WAIT_V(n) asm volatile("s_waitcnt vmcnt(" #n ")" ::: "memory")
#define WAIT_L(n) asm volatile("s_waitcnt lgkmcnt(" #n ")" ::: "memory")
#define BAR __builtin_amdgcn_s_barrier()
#define SCHED __builtin_amdgcn_sched_barrier(0)
  int tid = threadIdx.x;
  int wid = tid >> 6, lane = tid & 63, wr = wid >> 2, wc = wid & 3, fr = lane & 15, fq = lane >> 4;
  int bq = (lane >> 2) & 3, bp = lane & 3;
  int bbase = (fq * 8 + bq) * 256 + (wc * 4 + (bp >> 1)) * 16 + (bp & 1) * 8;
  int bxor = (bq | ((fq & 1) << 2)) << 5;
  f32x4 acc[2][2][4][2] = {};
  bf16x8 At[4][2], B0[2][2], B1[2][2];
  STAGE_B(tid, SB(0, 0), bcol, 0); STAGE_A(tid, SA(0, 0), brow, 0);
  STAGE_B(tid, SB(0, 1), bcol + HALF, 0); STAGE_A(tid, SA(0, 1), brow + HALF, 0);
  if (wr == 1) BAR;
  WAIT_V(4); BAR;
  STAGE_B(tid, SB(1, 0), bcol, 1); STAGE_A(tid, SA(1, 0), brow, 1); STAGE_B(tid, SB(1, 1), bcol + HALF, 1);
  WAIT_V(6); BAR;
  for (int t = 0; t < nt - 2; t += 2) {
    LDB(B0, 0, 0); SCHED; LDA(At, 0, 0); STAGE_A(tid, SA(1, 1), brow + HALF, t + 1);
    WAIT_L(8); BAR; WAIT_L(0); SCHED; MMA(0, 0, At, B0); BAR; SCHED;
    LDB(B1, 0, 1); STAGE_B(tid, SB(0, 0), bcol, t + 2);
    BAR; WAIT_L(0); SCHED; MMA(0, 1, At, B1); BAR;
    LDA(At, 0, 1); STAGE_A(tid, SA(0, 0), brow, t + 2);
    BAR; WAIT_L(0); SCHED; MMA(1, 0, At, B0); BAR; SCHED;
    STAGE_B(tid, SB(0, 1), bcol + HALF, t + 2);
    WAIT_V(6); BAR; MMA(1, 1, At, B1); BAR;
    LDB(B0, 1, 0); SCHED; LDA(At, 1, 0); STAGE_A(tid, SA(0, 1), brow + HALF, t + 2);
    WAIT_L(8); BAR; WAIT_L(0); SCHED; MMA(0, 0, At, B0); BAR; SCHED;
    LDB(B1, 1, 1); STAGE_B(tid, SB(1, 0), bcol, t + 3);
    BAR; WAIT_L(0); SCHED; MMA(0, 1, At, B1); BAR;
    LDA(At, 1, 1); STAGE_A(tid, SA(1, 0), brow, t + 3);
    BAR; WAIT_L(0); SCHED; MMA(1, 0, At, B0); BAR; SCHED;
    STAGE_B(tid, SB(1, 1), bcol + HALF, t + 3);
    WAIT_V(6); BAR; MMA(1, 1, At, B1); BAR;
  }
  int tz = threadIdx.x; asm volatile("" : "+v"(tz));
  wid = tz >> 6; lane = tz & 63; wr = wid >> 2; wc = wid & 3; fr = lane & 15; fq = lane >> 4;
  bq = (lane >> 2) & 3; bp = lane & 3;
  bbase = (fq * 8 + bq) * 256 + (wc * 4 + (bp >> 1)) * 16 + (bp & 1) * 8;
  bxor = (bq | ((fq & 1) << 2)) << 5;
  { LDB(B0, 0, 0); WAIT_V(0); LDA(At, 0, 0); STAGE_A(tz, SA(1, 1), brow + HALF, nt - 1);
    BAR; WAIT_L(0); SCHED; MMA(0, 0, At, B0); BAR;
    LDB(B1, 0, 1); BAR; WAIT_L(0); SCHED; MMA(0, 1, At, B1); BAR;
    LDA(At, 0, 1); WAIT_V(4); BAR; WAIT_L(0); SCHED; MMA(1, 0, At, B0); MMA(1, 1, At, B1); BAR; }
  { LDB(B0, 1, 0); LDA(At, 1, 0); WAIT_V(2); BAR; WAIT_L(0); SCHED; MMA(0, 0, At, B0); BAR;
    LDB(B1, 1, 1); WAIT_V(0); BAR; WAIT_L(0); SCHED; MMA(0, 1, At, B1); BAR;
    LDA(At, 1, 1); BAR; WAIT_L(0); SCHED; MMA(1, 0, At, B0); MMA(1, 1, At, B1); BAR; }
  if (wr == 0) BAR;
#pragma unroll
  for (int ai = 0; ai < 2; ++ai)
#pragma unroll
    for (int bj = 0; bj < 2; ++bj)
#pragma unroll
      for (int m = 0; m < 4; ++m)
#pragma unroll
        for (int n = 0; n < 2; ++n) {
          f32x4 a = acc[ai][bj][m][n];
          if constexpr (EPI == EPI_VT) {
            int d = bj * HALF + wc * 32 + n * 16 + fr;
            int key0 = brow + ai * HALF + wr * 64 + m * 16 + (((fq & 1) << 1) | (fq >> 1)) * 4;
            u32x2 w = {pk2(a[0], a[1]), pk2(a[2], a[3])};
            *reinterpret_cast<u32x2*>(out + (size_t)d * ldo + key0) = w;
          } else {
            int row = brow + ai * HALF + wr * 64 + m * 16 + fr;
            int col = bj * HALF + wc * 32 + n * 16 + fq * 4;
            if constexpr (EPI == EPI_MISH) {
              s16x4 bb = *reinterpret_cast<const s16x4*>(bias + bcol + col);
              float r[4];
#pragma unroll
              for (int j = 0; j < 4; ++j) r[j] = mish_ref(bfr(bfr(a[j]) + bf2f((u16)bb[j])));
              u32x2 w = {pk2(r[0], r[1]), pk2(r[2], r[3])};
              *reinterpret_cast<u32x2*>(out + (size_t)row * ldo + bcol + col) = w;
            } else {
              u32x2 w = {pk2(a[0], a[1]), pk2(a[2], a[3])};
              *reinterpret_cast<u32x2*>(out + (size_t)row * ldo + col) = w;
            }
          }
        }
#undef SA
#undef SB
#undef STAGE_A
#undef STAGE_B
#undef LDA
#undef LDB
#undef MMA
}

constexpr int N_KV_TILES = 256, N_Q_TILES = 128, N_UP_TILES = 56, N_GEMM_TILES = N_KV_TILES + N_Q_TILES + N_UP_TILES;

__global__ __launch_bounds__(512, 2) void proj_gemm_kernel(const u16* __restrict__ ckv, const u16* __restrict__ xn, const u16* __restrict__ wq,
                                                           const u16* __restrict__ wuk, const u16* __restrict__ wuv, const u16* __restrict__ w1,
                                                           const u16* __restrict__ b1, u16* __restrict__ Qo, u16* __restrict__ Ko,
                                                           u16* __restrict__ Vto, u16* __restrict__ acto) {
  extern __shared__ __attribute__((aligned(16))) char shm[];
  const int b = blockIdx.x;
  if (b < N_KV_TILES) {
    const int xcd = b & 7, i = b >> 3;
    const int pn = xcd * 4 + (i & 3), pm = i >> 2;
    if (pn < 16) gemm_tile<EPI_ROWMAJOR>(ckv, DC, wuk, HDALL, DC, DC / BK, pm * BM, pn * BM, Ko + (size_t)pn * SQ * HD, HD, nullptr, shm);
    else gemm_tile<EPI_VT>(ckv, DC, wuv, HDALL, DC, DC / BK, pm * BM, (pn - 16) * BM, Vto + (size_t)(pn - 16) * SQ * HD, SQ, nullptr, shm);
  } else if (b < N_KV_TILES + N_Q_TILES) {
    const int j = b - N_KV_TILES, xcd = j & 7, i = j >> 3;
    const int pn = xcd * 2 + (i & 1), pm = i >> 1;
    gemm_tile<EPI_ROWMAJOR>(xn, MDP, wq, HDALL, MD, MDP / BK, pm * BM, pn * BM, Qo + (size_t)pn * SQ * HD, HD, nullptr, shm);
  } else {
    const int j = b - N_KV_TILES - N_Q_TILES, pm = j & 7, pn = j >> 3;
    gemm_tile<EPI_MISH>(xn, MDP, w1, FF, MD, MDP / BK, pm * BM, pn * BM, acto, FF, b1, shm);
  }
}

constexpr int KT = 64;
constexpr int KBUF_B = KT * HD * 2;
constexpr int ATT_SHM = 4 * KBUF_B;
constexpr float SM_C = 0.0625f * 1.4426950408889634f;
constexpr float RESC_THR = 8.0f;

typedef __amdgpu_buffer_rsrc_t rsrc_t;
__device__ __forceinline__ rsrc_t mk_rsrc(const void* p, unsigned bytes) {
  return __builtin_amdgcn_make_buffer_rsrc((void*)p, 0, bytes, 0x00020000);
}
__device__ __forceinline__ bf16x8 bload16(rsrc_t r, unsigned voff, unsigned soff) {
  u32x4 v = __builtin_amdgcn_raw_buffer_load_b128(r, voff, soff, 0);
  return *reinterpret_cast<bf16x8*>(&v);
}

struct AttnState {
  f32x16 o[8];
  float m_run, l_run;
};

__global__ __launch_bounds__(256, 1) void attn_kernel(const u16* __restrict__ Q, const u16* __restrict__ K, const u16* __restrict__ Vt,
                                                      u16* __restrict__ ctx) {
  extern __shared__ __attribute__((aligned(16))) char shm[];
  const int b = blockIdx.x, xcd = b & 7, ii = b >> 3;
  const int h = xcd * 2 + (ii & 1), c = ii >> 1;
  const rsrc_t rK = mk_rsrc(K + (size_t)h * SQ * HD, SQ * HD * 2);
  const rsrc_t rV = mk_rsrc(Vt + (size_t)h * HD * SQ, SQ * HD * 2);
  const rsrc_t rQ = mk_rsrc(Q + (size_t)h * SQ * HD, SQ * HD * 2);
  LDS_AS char* lds = (LDS_AS char*)shm;
  constexpr int KB0 = 0, VB0 = 2 * KBUF_B;

  for (int pass = 0; pass < 2; ++pass) {
    int tid = threadIdx.x; asm volatile("" : "+v"(tid));
    const int wid = __builtin_amdgcn_readfirstlane(tid >> 6), lane = tid & 63, r32 = lane & 31, hi = lane >> 5;
    const int qs = wid & 1, kh = wid >> 1;
    const int chunk = pass == 0 ? c : 31 - c;
    const int q0 = chunk * 64 + qs * 32;
    const int ntile = chunk + 1;
    bf16x8 qf[16];
#pragma unroll
    for (int s = 0; s < 16; ++s) qf[s] = bload16(rQ, r32 * 512 + hi * 16, q0 * 512 + s * 32);
    f32x16 o[8];
#pragma unroll
    for (int d = 0; d < 8; ++d)
#pragma unroll
      for (int r = 0; r < 16; ++r) o[d][r] = 0.f;
    float m_run = -1e30f, l_run = 0.f;

    const unsigned kg_off = (tid >> 5) * 512 + (tid & 31) * 16;
    const unsigned vg_off = (tid >> 3) * 4096 + (tid & 7) * 16;
    int kw0 = (tid >> 5) * 512 + (((tid & 31) ^ (tid >> 5)) << 4);
    int kw1 = ((tid >> 5) + 8) * 512 + (((tid & 31) ^ ((tid >> 5) + 8)) << 4);
    int vw = VB0 + KBUF_B + (tid >> 3) * 128 + (((tid & 7) ^ ((tid >> 4) & 7)) << 4);
    int kra[8];
    const int krow = kh * 32 + r32;
#pragma unroll
    for (int s = 0; s < 8; ++s) kra[s] = krow * 512 + (((2 * s + hi) ^ (krow & 15)) << 4);
    int vra[2];
#pragma unroll
    for (int s2 = 0; s2 < 2; ++s2) vra[s2] = VB0 + r32 * 128 + (((kh * 4 + 2 * s2 + hi) ^ ((r32 >> 1) & 7)) << 4);

    bf16x8 ks[8], vs[8];
    auto gloadK = [&](int t) {
#pragma unroll
      for (int j = 0; j < 8; ++j) ks[j] = bload16(rK, kg_off, (t * KT + j * 8) * 512);
    };
    auto gloadV = [&](int t) {
#pragma unroll
      for (int j = 0; j < 8; ++j) vs[j] = bload16(rV, vg_off, j * 32 * 4096 + t * 128);
    };
    auto swriteK = [&](int boff) {
#pragma unroll
      for (int j = 0; j < 8; ++j) *(LDS_AS bf16x8*)(lds + boff + ((j & 1) ? kw1 : kw0) + (j >> 1) * 8192) = ks[j];
    };
    auto swriteV = [&](int boff) {
#pragma unroll
      for (int j = 0; j < 8; ++j) *(LDS_AS bf16x8*)(lds + boff + vw + j * 4096) = vs[j];
    };
    auto qk = [&](f32x16& sacc) {
#pragma unroll
      for (int r = 0; r < 16; ++r) sacc[r] = 0.f;
#pragma unroll
      for (int s = 0; s < 16; ++s) {
        bf16x8 kf = *(LDS_AS bf16x8*)(lds + kra[s & 7] + (s >> 3) * 256);
        sacc = __builtin_amdgcn_mfma_f32_32x32x16_bf16(kf, qf[s], sacc, 0, 0, 0);
      }
    };
    auto rowmax = [&](f32x16& sacc, int lim) {
#pragma unroll
      for (int r = 0; r < 16; ++r) sacc[r] = ((r & 3) + 8 * (r >> 2)) > lim ? -INFINITY : sacc[r];
      float pmax = sacc[0];
#pragma unroll
      for (int r = 1; r < 16; ++r) pmax = fmaxf(pmax, sacc[r]);
      pmax = fmaxf(pmax, __shfl_xor(pmax, 32));
      return pmax * SM_C;
    };
    const int dlim = r32 + (qs - kh) * 32 - 4 * hi;
    auto klim = [&](int t) { return t < chunk ? 64 : (t == chunk ? dlim : -1); };

    __syncthreads();
    const int last = ntile - 1;
    gloadK(0); gloadV(0); swriteK(0); swriteV(-KBUF_B);
    gloadK(last < 1 ? last : 1); swriteK(KBUF_B);
    __syncthreads();
    f32x16 sA, sB;
    qk(sA);
    m_run = fmaxf(m_run, rowmax(sA, klim(0)));
#pragma unroll
    for (int s = 0; s < 8; ++s) kra[s] ^= KBUF_B;
    for (int t = 0; t < ntile; ++t) {
      gloadK(t + 2 < last ? t + 2 : last); gloadV(t + 1 < last ? t + 1 : last);
      qk(sB);
      float ps = 0.f, p[16];
#pragma unroll
      for (int r = 0; r < 16; ++r) { p[r] = __builtin_amdgcn_exp2f(fmaf(sA[r], SM_C, -m_run)); ps += p[r]; }
      l_run += ps;
      bf16x8 pf[2];
#pragma unroll
      for (int s2 = 0; s2 < 2; ++s2) {
        u32x4 w = {pk2(p[8 * s2 + 0], p[8 * s2 + 1]), pk2(p[8 * s2 + 2], p[8 * s2 + 3]), pk2(p[8 * s2 + 4], p[8 * s2 + 5]),
                   pk2(p[8 * s2 + 6], p[8 * s2 + 7])};
        pf[s2] = *reinterpret_cast<bf16x8*>(&w);
      }
#pragma unroll
      for (int d = 0; d < 8; ++d)
#pragma unroll
        for (int s2 = 0; s2 < 2; ++s2) {
          bf16x8 vf = *(LDS_AS bf16x8*)(lds + vra[s2] + d * 4096);
          o[d] = __builtin_amdgcn_mfma_f32_32x32x16_bf16(vf, pf[s2], o[d], 0, 0, 0);
        }
      const float pmax = rowmax(sB, klim(t + 1));
      swriteK(0); swriteV(0);
      if (!__all(pmax - m_run <= RESC_THR)) {
        float mn = fmaxf(m_run, pmax);
        float alpha = __builtin_amdgcn_exp2f(m_run - mn);
        m_run = mn;
        l_run *= alpha;
#pragma unroll
        for (int d = 0; d < 8; ++d)
#pragma unroll
          for (int r = 0; r < 16; ++r) o[d][r] *= alpha;
      }
      __syncthreads();
      sA = sB;
#pragma unroll
      for (int s = 0; s < 8; ++s) kra[s] ^= KBUF_B;
      vra[0] ^= KBUF_B; vra[1] ^= KBUF_B; kw0 ^= KBUF_B; kw1 ^= KBUF_B; vw ^= KBUF_B;
    }
    __syncthreads();
    l_run += __shfl_xor(l_run, 32);
    float* mg = reinterpret_cast<float*>(shm) + qs * (130 * 64);
    if (kh == 1) {
#pragma unroll
      for (int d = 0; d < 8; ++d)
#pragma unroll
        for (int r = 0; r < 16; ++r) mg[(d * 16 + r) * 64 + lane] = o[d][r];
      mg[128 * 64 + lane] = m_run;
      mg[129 * 64 + lane] = l_run;
    }
    __syncthreads();
    if (kh == 0) {
      float m2 = mg[128 * 64 + lane], l2 = mg[129 * 64 + lane];
      float mn = fmaxf(m_run, m2);
      float a1 = __builtin_amdgcn_exp2f(m_run - mn), a2 = __builtin_amdgcn_exp2f(m2 - mn);
      float inv = 1.0f / (l_run * a1 + l2 * a2);
      a1 *= inv; a2 *= inv;
      u16* op = ctx + (size_t)(q0 + r32) * HDALL + h * HD + 4 * hi;
#pragma unroll
      for (int d = 0; d < 8; ++d)
#pragma unroll
        for (int g = 0; g < 4; ++g) {
          float v0 = o[d][4 * g + 0] * a1 + mg[(d * 16 + 4 * g + 0) * 64 + lane] * a2;
          float v1 = o[d][4 * g + 1] * a1 + mg[(d * 16 + 4 * g + 1) * 64 + lane] * a2;
          float v2 = o[d][4 * g + 2] * a1 + mg[(d * 16 + 4 * g + 2) * 64 + lane] * a2;
          float v3 = o[d][4 * g + 3] * a1 + mg[(d * 16 + 4 * g + 3) * 64 + lane] * a2;
          u32x2 w = {pk2(v0, v1), pk2(v2, v3)};
          *reinterpret_cast<u32x2*>(op + d * 32 + 8 * g) = w;
        }
    }
  }
}

__device__ __forceinline__ void fin_segment(const u16* __restrict__ A, int lda, const u16* __restrict__ Bg, int nk, int brow, int bcol,
                                            f32x4 (&acc)[2][2], char* shm, int tid) {
  const int wid = tid >> 6, lane = tid & 63, wr = wid >> 1, wc = wid & 1, fr = lane & 15, fq = lane >> 4;
  char* As = shm;
  char* Bs = shm + 16384;
  bf16x8 ar[2], br[2];
  auto gload = [&](int kt) {
#pragma unroll
    for (int j = 0; j < 2; ++j) {
      int idx = j * 256 + tid, r = idx >> 3, cc = idx & 7;
      ar[j] = *reinterpret_cast<const bf16x8*>(A + (size_t)(brow + r) * lda + kt * 64 + cc * 8);
      br[j] = *reinterpret_cast<const bf16x8*>(Bg + (size_t)(kt * 64 + r) * MD + bcol + cc * 8);
    }
  };
  auto swrite = [&](int buf) {
#pragma unroll
    for (int j = 0; j < 2; ++j) {
      int idx = j * 256 + tid, r = idx >> 3, cc = idx & 7;
      *reinterpret_cast<bf16x8*>(As + buf * 8192 + r * 128 + ((cc ^ ((r >> 1) & 7)) << 4)) = ar[j];
      int f = ((r >> 1) & 1) | (((r >> 3) & 1) << 1);
      *reinterpret_cast<bf16x8*>(Bs + buf * 8192 + r * 128 + ((cc ^ (f << 1)) << 4)) = br[j];
    }
  };
  const int bq = (lane >> 2) & 3, bp = lane & 3;
  __syncthreads();
  gload(0); swrite(0);
  __syncthreads();
  for (int kt = 0; kt < nk; ++kt) {
    const int buf = kt & 1;
    if (kt + 1 < nk) gload(kt + 1);
#pragma unroll
    for (int kk = 0; kk < 2; ++kk) {
      bf16x8 af[2], bf[2];
#pragma unroll
      for (int m = 0; m < 2; ++m) {
        int r = wr * 32 + m * 16 + fr;
        af[m] = *reinterpret_cast<const bf16x8*>(As + buf * 8192 + r * 128 + (((kk * 4 + fq) ^ ((r >> 1) & 7)) << 4));
      }
#pragma unroll
      for (int n = 0; n < 2; ++n) {
        int r = kk * 32 + fq * 8 + bq;
        int f = ((r >> 1) & 1) | (((r >> 3) & 1) << 1);
        int cp = wc * 2 + n;
        int off = buf * 8192 + r * 128 + (((cp * 2 + (bp >> 1)) ^ (f << 1)) << 4) + (bp & 1) * 8;
        LDS_AS char* p = (LDS_AS char*)Bs + off;
        s16x4 lo = __builtin_amdgcn_ds_read_tr16_b64_v4i16((LDS_AS s16x4*)p);
        s16x4 hh = __builtin_amdgcn_ds_read_tr16_b64_v4i16((LDS_AS s16x4*)(p + 512));
        bf[n] = (bf16x8){lo[0], lo[1], lo[2], lo[3], hh[0], hh[1], hh[2], hh[3]};
      }
#pragma unroll
      for (int m = 0; m < 2; ++m)
#pragma unroll
        for (int n = 0; n < 2; ++n) acc[m][n] = __builtin_amdgcn_mfma_f32_16x16x32_bf16(bf[n], af[m], acc[m][n], 0, 0, 0);
    }
    if (kt + 1 < nk) swrite(buf ^ 1);
    __syncthreads();
  }
}

__global__ __launch_bounds__(256) void final_kernel(const u16* __restrict__ ctx, const u16* __restrict__ wo, const u16* __restrict__ act,
                                                    const u16* __restrict__ w2, const u16* __restrict__ x, u16* __restrict__ out) {
  __shared__ __attribute__((aligned(16))) char shm[32768];
  const int b = blockIdx.x, pn = b % 7, pm = b / 7;
  const int brow = pm * 64, bcol = pn * 64, tid = threadIdx.x;
  f32x4 as[2][2] = {}, ag[2][2] = {};
  fin_segment(ctx, HDALL, wo, HDALL / 64, brow, bcol, as, shm, tid);
  fin_segment(act, FF, w2, FF / 64, brow, bcol, ag, shm, tid);
  const int wid = tid >> 6, lane = tid & 63, wr = wid >> 1, wc = wid & 1, fr = lane & 15, fq = lane >> 4;
#pragma unroll
  for (int m = 0; m < 2; ++m)
#pragma unroll
    for (int n = 0; n < 2; ++n) {
      int row = brow + wr * 32 + m * 16 + fr, col = bcol + wc * 32 + n * 16 + fq * 4;
      s16x4 xv = *reinterpret_cast<const s16x4*>(x + (size_t)row * MD + col);
      float r[4];
#pragma unroll
      for (int j = 0; j < 4; ++j) {
        float s = bfr(as[m][n][j]);
        float y = bfr(ag[m][n][j]);
        float g = bfr(y / (1.0f + expf(-y)));
        r[j] = bf2f((u16)xv[j]) + bfr(s * g);
      }
      u32x2 w = {pk2(r[0], r[1]), pk2(r[2], r[3])};
      *reinterpret_cast<u32x2*>(out + (size_t)row * MD + col) = w;
    }
}

extern "C" void kernel_launch(void* const* d_in, const int* in_sizes, int n_in, void* d_out, int out_size, void* d_ws, size_t ws_size,
                              hipStream_t stream) {
  const u16* x = (const u16*)d_in[0];
  const u16* ckv = (const u16*)d_in[1];
  const u16* wq = (const u16*)d_in[2];
  const u16* wuk = (const u16*)d_in[3];
  const u16* wuv = (const u16*)d_in[4];
  const u16* wo = (const u16*)d_in[5];
  const u16* w1 = (const u16*)d_in[7];
  const u16* b1 = (const u16*)d_in[8];
  const u16* w2 = (const u16*)d_in[9];
  char* ws = (char*)d_ws;
  u16* xn = (u16*)(ws + WS_XN); u16* Q = (u16*)(ws + WS_Q); u16* K = (u16*)(ws + WS_K); u16* Vt = (u16*)(ws + WS_VT);
  u16* act = (u16*)(ws + WS_ACT); u16* ctx = (u16*)(ws + WS_CTX);
  hipFuncSetAttribute((const void*)proj_gemm_kernel, hipFuncAttributeMaxDynamicSharedMemorySize, GEMM_SHM);
  hipFuncSetAttribute((const void*)attn_kernel, hipFuncAttributeMaxDynamicSharedMemorySize, ATT_SHM);
  ln_kernel<<<SQ / 4, 256, 0, stream>>>(x, xn);
  proj_gemm_kernel<<<N_GEMM_TILES, 512, GEMM_SHM, stream>>>(ckv, xn, wq, wuk, wuv, w1, b1, Q, K, Vt, act);
  attn_kernel<<<256, 256, ATT_SHM, stream>>>(Q, K, Vt, ctx);
  final_kernel<<<(SQ / 64) * 7, 256, 0, stream>>>(ctx, wo, act, w2, x, (u16*)d_out);
}
```

```cpp
#include <hip/hip_runtime.h>
#include <hip/hip_bf16.h>
#include <stdint.h>
#include <utility>

typedef short bf16x8 __attribute__((ext_vector_type(8)));
typedef short s16x4 __attribute__((ext_vector_type(4)));
typedef float f32x4 __attribute__((ext_vector_type(4)));
typedef float f32x16 __attribute__((ext_vector_type(16)));
typedef unsigned u32x2 __attribute__((ext_vector_type(2)));
typedef unsigned u32x4 __attribute__((ext_vector_type(4)));
typedef unsigned short u16;
#define LDS_AS __attribute__((address_space(3)))

constexpr int SQ = 2048, MD = 448, MDP = 512, DC = 1024, NH = 16, HD = 256, HDALL = NH * HD, FF = 1792;

constexpr size_t WS_XN = 0;
constexpr size_t WS_Q = WS_XN + (size_t)SQ * MDP * 2;
constexpr size_t WS_K = WS_Q + (size_t)NH * SQ * HD * 2;
constexpr size_t WS_VT = WS_K + (size_t)NH * SQ * HD * 2;
constexpr size_t WS_ACT = WS_VT + (size_t)NH * SQ * HD * 2;
constexpr size_t WS_CTX = WS_ACT + (size_t)SQ * FF * 2;
constexpr size_t WS_END = WS_CTX + (size_t)SQ * HDALL * 2;

__device__ __forceinline__ float bf2f(u16 v) { return __uint_as_float(((unsigned)v) << 16); }
__device__ __forceinline__ u16 f2bf(float f) { __hip_bfloat16 b = __float2bfloat16(f); return *reinterpret_cast<u16*>(&b); }
__device__ __forceinline__ float bfr(float f) { return bf2f(f2bf(f)); }
__device__ __forceinline__ unsigned pk2(float lo, float hi) {
  unsigned r; asm("v_cvt_pk_bf16_f32 %0, %1, %2" : "=v"(r) : "v"(lo), "v"(hi)); return r;
}


__device__ __forceinline__ float mish_ref(float h) {
  const float L2E = 1.4426950408889634f, LN2 = 0.6931471805599453f;
  float e = bfr(__builtin_amdgcn_exp2f(-fabsf(h) * L2E));
  float u = 1.0f + e;
  float lu = __builtin_amdgcn_logf(u) * LN2;
  float l = bfr(u == 1.0f ? e : lu * (e * __builtin_amdgcn_rcpf(u - 1.0f)));
  float sp = bfr(l + fmaxf(h, 0.f));
  float y = fminf(2.0f * sp, 80.0f);
  float w = __builtin_amdgcn_exp2f(y * L2E);
  float lw = __builtin_amdgcn_logf(w) * LN2;
  float em = (w == 1.0f) ? y : (w - 1.0f) * (y * __builtin_amdgcn_rcpf(lw));
  float th = bfr(em * __builtin_amdgcn_rcpf(em + 2.0f));
  return h * th;
}

__global__ __launch_bounds__(256) void ln_kernel(const u16* __restrict__ x, u16* __restrict__ xn) {
  const int row = blockIdx.x * 4 + (threadIdx.x >> 6), lane = threadIdx.x & 63;
  float v[8];
  const bool act = lane < 56;
  if (act) {
    bf16x8 r = *reinterpret_cast<const bf16x8*>(x + (size_t)row * MD + lane * 8);
#pragma unroll
    for (int j = 0; j < 8; ++j) v[j] = bf2f((u16)r[j]);
  } else {
#pragma unroll
    for (int j = 0; j < 8; ++j) v[j] = 0.f;
  }
  float s = 0.f;
#pragma unroll
  for (int j = 0; j < 8; ++j) s += v[j];
#pragma unroll
  for (int o = 32; o >= 1; o >>= 1) s += __shfl_xor(s, o);
  const float mean = s * (1.0f / MD);
  float q = 0.f;
  if (act) {
#pragma unroll
    for (int j = 0; j < 8; ++j) { float d = v[j] - mean; q += d * d; }
  }
#pragma unroll
  for (int o = 32; o >= 1; o >>= 1) q += __shfl_xor(q, o);
  const float mb = bfr(mean), vb = bfr(q * (1.0f / MD));
  const float den = bfr(sqrtf(bfr(vb + 1e-5f)));
  bf16x8 o8;
#pragma unroll
  for (int j = 0; j < 8; ++j) o8[j] = act ? (short)f2bf(bfr(v[j] - mb) / den) : (short)0;
  *reinterpret_cast<bf16x8*>(xn + (size_t)row * MDP + lane * 8) = o8;
}

constexpr int BM = 256, BK = 64, HALF = 128, HT = HALF * BK, GEMM_SHM = 8 * HT * 2;

__device__ __forceinline__ int lds_byte(int r, int c) {
  int st = (r >> 4) * 2 + (c >> 5), rr = r & 15, cc = c & 31, ob = rr * 64 + cc * 2;
  return st * 1024 + (ob ^ (((ob >> 9) & 1) << 5));
}
__device__ __forceinline__ void stage_rc(int b, int& R, int& C) {
  int st = b / 1024, sb = b % 1024, swz = sb ^ (((sb >> 9) & 1) << 5);
  R = (st >> 1) * 16 + swz / 64; C = (st & 1) * 32 + (swz % 64) / 2;
}

enum { EPI_ROWMAJOR = 0, EPI_VT = 1, EPI_MISH = 2 };

template <int EPI>
__device__ __forceinline__ void gemm_tile(const u16* __restrict__ A, int lda, const u16* __restrict__ Bg, int ldb, int kmax, int nt,
                                          int brow, int bcol, u16* __restrict__ out, int ldo, const u16* __restrict__ bias, char* shm_raw) {
  u16* shm = (u16*)shm_raw;
#define SA(b, h) (shm + ((b) * 2 + (h)) * HT)
#define SB(b, h) (shm + (4 + (b) * 2 + (h)) * HT)
#define STAGE_A(T, P, br, kt) do { const u16* _g = A + (long)(br) * lda + (long)(kt) * BK;                     \
    for (int _i = 0; _i < 2; ++_i) { int _b = (T) * 16 + _i * 8192; int _r, _c; stage_rc(_b, _r, _c);          \
      __builtin_amdgcn_global_load_lds((const unsigned*)(_g + (long)_r * lda + _c), (LDS_AS unsigned*)((char*)(P) + _b), 16, 0, 0); } } while (0)
#define STAGE_B(T, P, bc, kt) do {                                                                              \
    for (int _i = 0; _i < 2; ++_i) { int _b = (T) * 16 + _i * 8192; int _k = _b >> 8, _pc = (_b >> 4) & 15;     \
      int _c = _pc ^ (((_k & 3) | (((_k >> 3) & 1) << 2)) << 1); int _kr = (kt) * BK + _k; _kr = _kr < kmax ? _kr : kmax - 1; \
      __builtin_amdgcn_global_load_lds((const unsigned*)(Bg + (long)_kr * ldb + (bc) + _c * 8), (LDS_AS unsigned*)((char*)(P) + _b), 16, 0, 0); } } while (0)
#define LDA(dst, b, h) for (int m = 0; m < 4; ++m) for (int k = 0; k < 2; ++k) \
    dst[m][k] = *reinterpret_cast<const bf16x8*>((char*)SA(b, h) + lds_byte(wr * 64 + m * 16 + fr, k * 32 + fq * 8))
#define LDB(dst, b, h) for (int n = 0; n < 2; ++n) for (int k = 0; k < 2; ++k) {                                \
    LDS_AS char* _p = (LDS_AS char*)SB(b, h) + bbase + n * 32 + k * (32 * 256);                                 \
    _p = (LDS_AS char*)((unsigned)(uintptr_t)(LDS_AS char*)SB(b, h) + ((bbase + n * 32) ^ bxor) + k * (32 * 256)); \
    s16x4 _lo = __builtin_amdgcn_ds_read_tr16_b64_v4i16((LDS_AS s16x4*)_p);                                     \
    s16x4 _hi = __builtin_amdgcn_ds_read_tr16_b64_v4i16((LDS_AS s16x4*)(_p + 1024));                            \
    dst[n][k] = (bf16x8){_lo[0], _lo[1], _lo[2], _lo[3], _hi[0], _hi[1], _hi[2], _hi[3]}; }
#define MMA(ai, bj, At, Bt) do { __builtin_amdgcn_s_setprio(1);                                                 \
    for (int m = 0; m < 4; ++m) for (int n = 0; n < 2; ++n) for (int k = 0; k < 2; ++k) {                         \
      if constexpr (EPI == EPI_VT) acc[ai][bj][m][n] = __builtin_amdgcn_mfma_f32_16x16x32_bf16(At[m][k], Bt[n][k], acc[ai][bj][m][n], 0, 0, 0); \
      else acc[ai][bj][m][n] = __builtin_amdgcn_mfma_f32_16x16x32_bf16(Bt[n][k], At[m][k], acc[ai][bj][m][n], 0, 0, 0); }                     \
    __builtin_amdgcn_s_setprio(0); } while (0)
#define WAIT_V(n) asm volatile("s_waitcnt vmcnt(" #n ")" ::: "memory")
#define WAIT_L(n) asm volatile("s_waitcnt lgkmcnt(" #n ")" ::: "memory")
#define BAR __builtin_amdgcn_s_barrier()
#define SCHED __builtin_amdgcn_sched_barrier(0)
  int tid = threadIdx.x;
  int wid = tid >> 6, lane = tid & 63, wr = wid >> 2, wc = wid & 3, fr = lane & 15, fq = lane >> 4;
  int bq = (lane >> 2) & 3, bp = lane & 3;
  int bbase = (fq * 8 + bq) * 256 + (wc * 4 + (bp >> 1)) * 16 + (bp & 1) * 8;
  int bxor = (bq | ((fq & 1) << 2)) << 5;
  f32x4 acc[2][2][4][2] = {};
  bf16x8 At[4][2], B0[2][2], B1[2][2];
  STAGE_B(tid, SB(0, 0), bcol, 0); STAGE_A(tid, SA(0, 0), brow, 0);
  STAGE_B(tid, SB(0, 1), bcol + HALF, 0); STAGE_A(tid, SA(0, 1), brow + HALF, 0);
  if (wr == 1) BAR;
  WAIT_V(4); BAR;
  STAGE_B(tid, SB(1, 0), bcol, 1); STAGE_A(tid, SA(1, 0), brow, 1); STAGE_B(tid, SB(1, 1), bcol + HALF, 1);
  WAIT_V(6); BAR;
  for (int t = 0; t < nt - 2; t += 2) {
    LDB(B0, 0, 0); SCHED; LDA(At, 0, 0); STAGE_A(tid, SA(1, 1), brow + HALF, t + 1);
    WAIT_L(8); BAR; WAIT_L(0); SCHED; MMA(0, 0, At, B0); BAR; SCHED;
    LDB(B1, 0, 1); STAGE_B(tid, SB(0, 0), bcol, t + 2);
    BAR; WAIT_L(0); SCHED; MMA(0, 1, At, B1); BAR;
    LDA(At, 0, 1); STAGE_A(tid, SA(0, 0), brow, t + 2);
    BAR; WAIT_L(0); SCHED; MMA(1, 0, At, B0); BAR; SCHED;
    STAGE_B(tid, SB(0, 1), bcol + HALF, t + 2);
    WAIT_V(6); BAR; MMA(1, 1, At, B1); BAR;
    LDB(B0, 1, 0); SCHED; LDA(At, 1, 0); STAGE_A(tid, SA(0, 1), brow + HALF, t + 2);
    WAIT_L(8); BAR; WAIT_L(0); SCHED; MMA(0, 0, At, B0); BAR; SCHED;
    LDB(B1, 1, 1); STAGE_B(tid, SB(1, 0), bcol, t + 3);
    BAR; WAIT_L(0); SCHED; MMA(0, 1, At, B1); BAR;
    LDA(At, 1, 1); STAGE_A(tid, SA(1, 0), brow, t + 3);
    BAR; WAIT_L(0); SCHED; MMA(1, 0, At, B0); BAR; SCHED;
    STAGE_B(tid, SB(1, 1), bcol + HALF, t + 3);
    WAIT_V(6); BAR; MMA(1, 1, At, B1); BAR;
  }
  int tz = threadIdx.x; asm volatile("" : "+v"(tz));
  wid = tz >> 6; lane = tz & 63; wr = wid >> 2; wc = wid & 3; fr = lane & 15; fq = lane >> 4;
  bq = (lane >> 2) & 3; bp = lane & 3;
  bbase = (fq * 8 + bq) * 256 + (wc * 4 + (bp >> 1)) * 16 + (bp & 1) * 8;
  bxor = (bq | ((fq & 1) << 2)) << 5;
  { LDB(B0, 0, 0); WAIT_V(0); LDA(At, 0, 0); STAGE_A(tz, SA(1, 1), brow + HALF, nt - 1);
    BAR; WAIT_L(0); SCHED; MMA(0, 0, At, B0); BAR;
    LDB(B1, 0, 1); BAR; WAIT_L(0); SCHED; MMA(0, 1, At, B1); BAR;
    LDA(At, 0, 1); WAIT_V(4); BAR; WAIT_L(0); SCHED; MMA(1, 0, At, B0); MMA(1, 1, At, B1); BAR; }
  { LDB(B0, 1, 0); LDA(At, 1, 0); WAIT_V(2); BAR; WAIT_L(0); SCHED; MMA(0, 0, At, B0); BAR;
    LDB(B1, 1, 1); WAIT_V(0); BAR; WAIT_L(0); SCHED; MMA(0, 1, At, B1); BAR;
    LDA(At, 1, 1); BAR; WAIT_L(0); SCHED; MMA(1, 0, At, B0); MMA(1, 1, At, B1); BAR; }
  if (wr == 0) BAR;
#pragma unroll
  for (int ai = 0; ai < 2; ++ai)
#pragma unroll
    for (int bj = 0; bj < 2; ++bj)
#pragma unroll
      for (int m = 0; m < 4; ++m)
#pragma unroll
        for (int n = 0; n < 2; ++n) {
          f32x4 a = acc[ai][bj][m][n];
          if constexpr (EPI == EPI_VT) {
            int d = bj * HALF + wc * 32 + n * 16 + fr;
            int key0 = brow + ai * HALF + wr * 64 + m * 16 + (((fq & 1) << 1) | (fq >> 1)) * 4;
            u32x2 w = {pk2(a[0], a[1]), pk2(a[2], a[3])};
            *reinterpret_cast<u32x2*>(out + (size_t)(key0 >> 6) * (HD * 64) + d * 64 + (key0 & 63)) = w;
          } else {
            int row = brow + ai * HALF + wr * 64 + m * 16 + fr;
            int col = bj * HALF + wc * 32 + n * 16 + fq * 4;
            if constexpr (EPI == EPI_MISH) {
              s16x4 bb = *reinterpret_cast<const s16x4*>(bias + bcol + col);
              float r[4];
#pragma unroll
              for (int j = 0; j < 4; ++j) r[j] = mish_ref(bfr(bfr(a[j]) + bf2f((u16)bb[j])));
              u32x2 w = {pk2(r[0], r[1]), pk2(r[2], r[3])};
              *reinterpret_cast<u32x2*>(out + (size_t)row * ldo + bcol + col) = w;
            } else {
              u32x2 w = {pk2(a[0], a[1]), pk2(a[2], a[3])};
              *reinterpret_cast<u32x2*>(out + (size_t)row * ldo + col) = w;
            }
          }
        }
#undef SA
#undef SB
#undef STAGE_A
#undef STAGE_B
#undef LDA
#undef LDB
#undef MMA
}

constexpr int N_KV_TILES = 256, N_Q_TILES = 128, N_UP_TILES = 56, N_GEMM_TILES = N_KV_TILES + N_Q_TILES + N_UP_TILES;

__global__ __launch_bounds__(512, 2) void proj_gemm_kernel(const u16* __restrict__ ckv, const u16* __restrict__ xn, const u16* __restrict__ wq,
                                                           const u16* __restrict__ wuk, const u16* __restrict__ wuv, const u16* __restrict__ w1,
                                                           const u16* __restrict__ b1, u16* __restrict__ Qo, u16* __restrict__ Ko,
                                                           u16* __restrict__ Vto, u16* __restrict__ acto) {
  extern __shared__ __attribute__((aligned(16))) char shm[];
  const int b = blockIdx.x;
  if (b < N_KV_TILES) {
    const int xcd = b & 7, i = b >> 3;
    const int pn = xcd * 4 + (i & 3), pm = i >> 2;
    if (pn < 16) gemm_tile<EPI_ROWMAJOR>(ckv, DC, wuk, HDALL, DC, DC / BK, pm * BM, pn * BM, Ko + (size_t)pn * SQ * HD, HD, nullptr, shm);
    else gemm_tile<EPI_VT>(ckv, DC, wuv, HDALL, DC, DC / BK, pm * BM, (pn - 16) * BM, Vto + (size_t)(pn - 16) * SQ * HD, SQ, nullptr, shm);
  } else if (b < N_KV_TILES + N_Q_TILES) {
    const int j = b - N_KV_TILES, xcd = j & 7, i = j >> 3;
    const int pn = xcd * 2 + (i & 1), pm = i >> 1;
    gemm_tile<EPI_ROWMAJOR>(xn, MDP, wq, HDALL, MD, MDP / BK, pm * BM, pn * BM, Qo + (size_t)pn * SQ * HD, HD, nullptr, shm);
  } else {
    const int j = b - N_KV_TILES - N_Q_TILES, pm = j & 7, pn = j >> 3;
    gemm_tile<EPI_MISH>(xn, MDP, w1, FF, MD, MDP / BK, pm * BM, pn * BM, acto, FF, b1, shm);
  }
}

constexpr int KT = 64;
constexpr int KBUF_B = KT * HD * 2;
constexpr int ATT_SHM = 4 * KBUF_B;
constexpr float SM_C = 0.0625f * 1.4426950408889634f;
constexpr float RESC_THR = 8.0f;

typedef __amdgpu_buffer_rsrc_t rsrc_t;
__device__ __forceinline__ rsrc_t mk_rsrc(const void* p, unsigned bytes) {
  return __builtin_amdgcn_make_buffer_rsrc((void*)p, 0, bytes, 0x00020000);
}
__device__ __forceinline__ bf16x8 bload16(rsrc_t r, unsigned voff, unsigned soff) {
  u32x4 v = __builtin_amdgcn_raw_buffer_load_b128(r, voff, soff, 0);
  return *reinterpret_cast<bf16x8*>(&v);
}

__device__ __forceinline__ void bdma16(rsrc_t r, unsigned voff, unsigned soff, unsigned lds_dst) {
  asm volatile("s_mov_b32 m0, %0\n\ts_nop 0\n\tbuffer_load_dwordx4 %1, %2, %3 offen lds" :: "s"(lds_dst), "v"(voff), "s"(r), "s"(soff) : "m0", "memory");
}

template <int I> __device__ __forceinline__ void acc_zero1() { asm volatile("v_accvgpr_write_b32 a[%c0], 0" :: "i"(I)); }
template <int... Is> __device__ __forceinline__ void acc_zero(std::integer_sequence<int, Is...>) { (acc_zero1<Is>(), ...); }
template <int I> __device__ __forceinline__ void acc_scale1(float alpha) {
  float tmp;
  asm volatile("v_accvgpr_read_b32 %0, a[%c2]\n\tv_mul_f32 %0, %0, %1\n\tv_accvgpr_write_b32 a[%c2], %0" : "=&v"(tmp) : "v"(alpha), "i"(I));
}
template <int... Is> __device__ __forceinline__ void acc_scale(float alpha, std::integer_sequence<int, Is...>) { (acc_scale1<Is>(alpha), ...); }
template <int I> __device__ __forceinline__ float acc_read() { float x; asm volatile("v_accvgpr_read_b32 %0, a[%c1]" : "=v"(x) : "i"(I)); return x; }
template <int S> __device__ __forceinline__ void q_load1(rsrc_t r, unsigned voff, unsigned soff) {
  asm volatile("buffer_load_dwordx4 a[%c3:%c4], %0, %1, %2 offen" :: "v"(voff), "s"(r), "s"(soff + S * 32), "i"(128 + 4 * S), "i"(131 + 4 * S) : "memory");
}
template <int... Ss> __device__ __forceinline__ void q_load(rsrc_t r, unsigned voff, unsigned soff, std::integer_sequence<int, Ss...>) { (q_load1<Ss>(r, voff, soff), ...); }
template <int S> __device__ __forceinline__ void qk1(f32x16& acc, const f32x16& cin, bf16x8 k) {
  if constexpr (S == 0) asm volatile("s_nop 1\n\tv_mfma_f32_32x32x16_bf16 %0, %1, a[128:131], %2" : "=&v"(acc) : "v"(k), "v"(cin));
  else asm volatile("v_mfma_f32_32x32x16_bf16 %0, %1, a[%c2:%c3], %0" : "+v"(acc) : "v"(k), "i"(128 + 4 * S), "i"(131 + 4 * S));
}
template <int... Ss> __device__ __forceinline__ void qk_all(f32x16& acc, const f32x16& cin, const bf16x8 (&kf)[16], std::integer_sequence<int, Ss...>) {
  (qk1<Ss>(acc, cin, kf[Ss]), ...);
}
template <int I> __device__ __forceinline__ void pv1(bf16x8 v, bf16x8 pfrag) {
  constexpr int D = I >> 1;
  if constexpr (I == 0) asm volatile("s_nop 1\n\tv_mfma_f32_32x32x16_bf16 a[%c2:%c3], %0, %1, a[%c2:%c3]" :: "v"(v), "v"(pfrag), "i"(D * 16), "i"(D * 16 + 15));
  else asm volatile("v_mfma_f32_32x32x16_bf16 a[%c2:%c3], %0, %1, a[%c2:%c3]" :: "v"(v), "v"(pfrag), "i"(D * 16), "i"(D * 16 + 15));
}
template <int I0, int... Is> __device__ __forceinline__ void pv_rng(const bf16x8 (&vf)[16], const bf16x8 (&pf)[2], std::integer_sequence<int, Is...>) {
  (pv1<I0 + Is>(vf[I0 + Is], pf[(I0 + Is) & 1]), ...);
}
template <int R> __device__ __forceinline__ f32x4 acc_read4() { return (f32x4){acc_read<R>(), acc_read<R + 1>(), acc_read<R + 2>(), acc_read<R + 3>()}; }
template <int KEEP0, int... Gs> __device__ __forceinline__ void epi_send(LDS_AS char* xb, int lane, std::integer_sequence<int, Gs...>) {
  ((*(LDS_AS f32x4*)(xb + Gs * 1024 + lane * 16) = acc_read4<(64 - KEEP0) + 4 * Gs>()), ...);
}
template <int KEEP0, int G> __device__ __forceinline__ void epi_comb1(LDS_AS char* xp, LDS_AS char* ot, int lane, float a1, float a2) {
  f32x4 o1 = acc_read4<KEEP0 + 4 * G>();
  f32x4 o2 = *(LDS_AS f32x4*)(xp + G * 1024 + lane * 16);
  u32x2 w = {pk2(o1[0] * a1 + o2[0] * a2, o1[1] * a1 + o2[1] * a2), pk2(o1[2] * a1 + o2[2] * a2, o1[3] * a1 + o2[3] * a2)};
  *(LDS_AS u32x2*)(ot + (lane & 31) * 272 + ((G >> 2) * 32 + 8 * (G & 3) + 4 * (lane >> 5)) * 2) = w;
}
template <int KEEP0, int... Gs> __device__ __forceinline__ void epi_comb(LDS_AS char* xp, LDS_AS char* ot, int lane, float a1, float a2, std::integer_sequence<int, Gs...>) {
  (epi_comb1<KEEP0, Gs>(xp, ot, lane, a1, a2), ...);
}
template <int N> using iseq = std::make_integer_sequence<int, N>;
template <int J, int WAITN> __device__ __forceinline__ void stage_step(unsigned waddr, rsrc_t r, unsigned voff, unsigned soff) {
  constexpr int OFF = J < 8 ? (J >> 1) * 8192 : (J - 8) * 4096;
  asm volatile("s_waitcnt vmcnt(%c0)\n\tds_write_b128 %1, a[%c2:%c3] offset:%c4\n\tbuffer_load_dwordx4 a[%c2:%c3], %5, %6, %7 offen"
               :: "i"(WAITN), "v"(waddr), "i"(192 + 4 * J), "i"(195 + 4 * J), "i"(OFF), "v"(voff), "s"(r), "s"(soff) : "memory");
}
template <int J> __device__ __forceinline__ void stage_load(rsrc_t r, unsigned voff, unsigned soff) {
  asm volatile("buffer_load_dwordx4 a[%c0:%c1], %2, %3, %4 offen" :: "i"(192 + 4 * J), "i"(195 + 4 * J), "v"(voff), "s"(r), "s"(soff) : "memory");
}
template <int J> __device__ __forceinline__ void stage_write(unsigned waddr) {
  constexpr int OFF = J < 8 ? (J >> 1) * 8192 : (J - 8) * 4096;
  asm volatile("ds_write_b128 %0, a[%c1:%c2] offset:%c3" :: "v"(waddr), "i"(192 + 4 * J), "i"(195 + 4 * J), "i"(OFF) : "memory");
}

__global__ __launch_bounds__(256, 1) void attn_kernel(const u16* __restrict__ Q, const u16* __restrict__ K, const u16* __restrict__ Vt,
                                                      u16* __restrict__ ctx) {
  extern __shared__ __attribute__((aligned(16))) char shm[];
  const int b = blockIdx.x, xcd = b & 7, ii = b >> 3;
  const int h = xcd * 2 + (ii & 1), c = ii >> 1;
  const rsrc_t rK = mk_rsrc(K + (size_t)h * SQ * HD, SQ * HD * 2);
  const rsrc_t rV = mk_rsrc(Vt + (size_t)h * HD * SQ, SQ * HD * 2);
  const rsrc_t rQ = mk_rsrc(Q + (size_t)h * SQ * HD, SQ * HD * 2);
  LDS_AS char* lds = (LDS_AS char*)shm;
  const unsigned lds0 = (unsigned)(uintptr_t)lds;
  constexpr int VB0 = 2 * KBUF_B;
  asm volatile("" ::: "a0", "a63", "a127", "a128", "a191", "a255");

  for (int pass = 0; pass < 2; ++pass) {
    int tid = threadIdx.x; asm volatile("" : "+v"(tid));
    const int wid = __builtin_amdgcn_readfirstlane(tid >> 6), lane = tid & 63, r32 = lane & 31, hi = lane >> 5;
    const int qs = wid & 1, kh = wid >> 1;
    const int chunk = pass == 0 ? c : 31 - c;
    const int q0 = chunk * 64 + qs * 32;
    const int ntile = chunk + 1, last = chunk;
    float m_run = -1e30f, l_run = 0.f;

    const unsigned kg = (tid >> 5) * 512 + (tid & 31) * 16;
    const unsigned vg = (tid >> 3) * 128 + (tid & 7) * 16;
    unsigned kw0 = lds0 + (tid >> 5) * 512 + (((tid & 31) ^ (tid >> 5)) << 4);
    unsigned kw1 = lds0 + ((tid >> 5) + 8) * 512 + (((tid & 31) ^ ((tid >> 5) + 8)) << 4);
    unsigned vw = lds0 + VB0 + (tid >> 3) * 128 + (((tid & 7) ^ ((tid >> 4) & 7)) << 4);
    int kra[8];
    const int krow = kh * 32 + r32;
#pragma unroll
    for (int s = 0; s < 8; ++s) kra[s] = krow * 512 + (((2 * s + hi) ^ (krow & 15)) << 4);
    int vra[2];
#pragma unroll
    for (int s2 = 0; s2 < 2; ++s2) vra[s2] = VB0 + r32 * 128 + (((kh * 4 + 2 * s2 + hi) ^ ((r32 >> 1) & 7)) << 4);

#define SB0() __builtin_amdgcn_sched_barrier(0)
    auto rowmax = [&](const f32x16& sacc) {
      float pmax = sacc[0];
#pragma unroll
      for (int r = 1; r < 16; ++r) pmax = fmaxf(pmax, sacc[r]);
      auto rr = __builtin_amdgcn_permlane32_swap(__float_as_uint(pmax), __float_as_uint(pmax), false, false);
      pmax = fmaxf(__uint_as_float(rr[0]), __uint_as_float(rr[1]));
      return pmax * SM_C;
    };
    const int dlim = r32 + (qs - kh) * 32 - 4 * hi;
    f32x16 cinit;
    auto set_mask = [&](int t) {
      const int lim = t < chunk ? 64 : (t == chunk ? dlim : -1);
#pragma unroll
      for (int r = 0; r < 16; ++r) cinit[r] = ((r & 3) + 8 * (r >> 2)) > lim ? -INFINITY : 0.f;
    };
    auto ldK = [&](int t) { const unsigned so = t * KBUF_B;
      stage_load<0>(rK, kg, so); stage_load<1>(rK, kg, so + 4096); stage_load<2>(rK, kg, so + 8192); stage_load<3>(rK, kg, so + 12288);
      stage_load<4>(rK, kg, so + 16384); stage_load<5>(rK, kg, so + 20480); stage_load<6>(rK, kg, so + 24576); stage_load<7>(rK, kg, so + 28672); };
    auto ldV = [&](int t) { const unsigned so = t * KBUF_B;
      stage_load<8>(rV, vg, so); stage_load<9>(rV, vg, so + 4096); stage_load<10>(rV, vg, so + 8192); stage_load<11>(rV, vg, so + 12288);
      stage_load<12>(rV, vg, so + 16384); stage_load<13>(rV, vg, so + 20480); stage_load<14>(rV, vg, so + 24576); stage_load<15>(rV, vg, so + 28672); };
    auto wrK = [&](unsigned x) { stage_write<0>(kw0 ^ x); stage_write<1>(kw1 ^ x); stage_write<2>(kw0 ^ x); stage_write<3>(kw1 ^ x);
      stage_write<4>(kw0 ^ x); stage_write<5>(kw1 ^ x); stage_write<6>(kw0 ^ x); stage_write<7>(kw1 ^ x); };
    auto wrV = [&](unsigned x) { stage_write<8>(vw ^ x); stage_write<9>(vw ^ x); stage_write<10>(vw ^ x); stage_write<11>(vw ^ x);
      stage_write<12>(vw ^ x); stage_write<13>(vw ^ x); stage_write<14>(vw ^ x); stage_write<15>(vw ^ x); };

    __syncthreads();
    ldK(0); ldV(0);
    q_load(rQ, r32 * 512 + hi * 16, q0 * 512, iseq<16>{});
    acc_zero(iseq<128>{});
    asm volatile("s_waitcnt vmcnt(16)" ::: "memory");
    wrK(0); wrV(0);
    ldK(last < 1 ? last : 1);
    asm volatile("s_waitcnt vmcnt(0)" ::: "memory");
    wrK(KBUF_B);
    ldK(last < 2 ? last : 2); ldV(last < 1 ? last : 1);
    asm volatile("s_waitcnt lgkmcnt(0)" ::: "memory");
    __builtin_amdgcn_s_barrier();
    f32x16 sA, sB;
    set_mask(0);
    {
      bf16x8 kf[16];
#pragma unroll
      for (int s = 0; s < 16; ++s) kf[s] = *(LDS_AS bf16x8*)(lds + kra[s & 7] + (s >> 3) * 256);
      qk_all(sA, cinit, kf, iseq<16>{});
      asm volatile("s_nop 15\n\ts_nop 15" : "+v"(sA));
    }
    m_run = fmaxf(m_run, rowmax(sA));
    set_mask(1);
    vw ^= KBUF_B;
    SB0();
    __builtin_amdgcn_s_barrier();
    for (int t = 0; t < ntile; ++t) {
      const int kb = ((t + 1) & 1) * KBUF_B, vb = (t & 1) * KBUF_B;
      const unsigned ks = (t + 3 < last ? t + 3 : last) * KBUF_B, vs = (t + 2 < last ? t + 2 : last) * KBUF_B;
      bf16x8 kf[16], vf[16];
#pragma unroll
      for (int s = 0; s < 16; ++s) kf[s] = *(LDS_AS bf16x8*)(lds + kb + kra[s & 7] + (s >> 3) * 256);
      SB0();
      qk1<0>(sB, cinit, kf[0]);  qk1<1>(sB, cinit, kf[1]);   stage_step<0, 15>(kw0, rK, kg, ks);
      qk1<2>(sB, cinit, kf[2]);  qk1<3>(sB, cinit, kf[3]);   stage_step<1, 15>(kw1, rK, kg, ks + 4096);
      qk1<4>(sB, cinit, kf[4]);  qk1<5>(sB, cinit, kf[5]);   stage_step<2, 15>(kw0, rK, kg, ks + 8192);
      qk1<6>(sB, cinit, kf[6]);  qk1<7>(sB, cinit, kf[7]);   stage_step<3, 15>(kw1, rK, kg, ks + 12288);
      qk1<8>(sB, cinit, kf[8]);  qk1<9>(sB, cinit, kf[9]);   stage_step<4, 15>(kw0, rK, kg, ks + 16384);
      qk1<10>(sB, cinit, kf[10]); qk1<11>(sB, cinit, kf[11]); stage_step<5, 15>(kw1, rK, kg, ks + 20480);
      qk1<12>(sB, cinit, kf[12]); qk1<13>(sB, cinit, kf[13]); stage_step<6, 15>(kw0, rK, kg, ks + 24576);
      qk1<14>(sB, cinit, kf[14]); qk1<15>(sB, cinit, kf[15]); stage_step<7, 15>(kw1, rK, kg, ks + 28672);
      float ps = 0.f, p[16];
#pragma unroll
      for (int r = 0; r < 16; ++r) { p[r] = __builtin_amdgcn_exp2f(fmaf(sA[r], SM_C, -m_run)); ps += p[r]; }
      l_run += ps;
      bf16x8 pf[2];
#pragma unroll
      for (int s2 = 0; s2 < 2; ++s2) {
        u32x4 w = {pk2(p[8 * s2 + 0], p[8 * s2 + 1]), pk2(p[8 * s2 + 2], p[8 * s2 + 3]), pk2(p[8 * s2 + 4], p[8 * s2 + 5]),
                   pk2(p[8 * s2 + 6], p[8 * s2 + 7])};
        pf[s2] = *reinterpret_cast<bf16x8*>(&w);
      }
#pragma unroll
      for (int d = 0; d < 8; ++d)
#pragma unroll
        for (int s2 = 0; s2 < 2; ++s2) vf[d * 2 + s2] = *(LDS_AS bf16x8*)(lds + vb + vra[s2] + d * 4096);
      SB0();
      pv1<0>(vf[0], pf[0]); pv1<1>(vf[1], pf[1]); stage_step<8, 15>(vw, rV, vg, vs);
      pv1<2>(vf[2], pf[0]);
      asm volatile("" : "+v"(sB));
      float pmax = rowmax(sB);
      pv1<3>(vf[3], pf[1]);   stage_step<9, 15>(vw, rV, vg, vs + 4096);
      pv1<4>(vf[4], pf[0]);  pv1<5>(vf[5], pf[1]);   stage_step<10, 15>(vw, rV, vg, vs + 8192);
      pv1<6>(vf[6], pf[0]);  pv1<7>(vf[7], pf[1]);   stage_step<11, 15>(vw, rV, vg, vs + 12288);
      pv1<8>(vf[8], pf[0]);  pv1<9>(vf[9], pf[1]);   stage_step<12, 15>(vw, rV, vg, vs + 16384);
      pv1<10>(vf[10], pf[0]);
      asm volatile("" : "+v"(pmax));
      pv1<11>(vf[11], pf[1]); stage_step<13, 15>(vw, rV, vg, vs + 20480);
      pv1<12>(vf[12], pf[0]); pv1<13>(vf[13], pf[1]); stage_step<14, 15>(vw, rV, vg, vs + 24576);
      pv1<14>(vf[14], pf[0]); pv1<15>(vf[15], pf[1]); stage_step<15, 15>(vw, rV, vg, vs + 28672);
      SB0();
      if (!__all(pmax - m_run <= RESC_THR)) {
        float mn = fmaxf(m_run, pmax);
        float alpha = __builtin_amdgcn_exp2f(m_run - mn);
        m_run = mn;
        l_run *= alpha;
        asm volatile("s_nop 15\n\ts_nop 15" ::: "memory");
        acc_scale(alpha, iseq<128>{});
        asm volatile("s_nop 3" ::: "memory");
      }
      if (t + 2 >= chunk) set_mask(t + 2);
      sA = sB;
      kw0 ^= KBUF_B; kw1 ^= KBUF_B; vw ^= KBUF_B;
      SB0();
      asm volatile("s_waitcnt lgkmcnt(0)" ::: "memory");
      __builtin_amdgcn_s_barrier();
    }
    asm volatile("s_waitcnt vmcnt(0)" ::: "memory");
    asm volatile("s_nop 15\n\ts_nop 15" ::: "memory");
    {
      auto rr = __builtin_amdgcn_permlane32_swap(__float_as_uint(l_run), __float_as_uint(l_run), false, false);
      l_run = __uint_as_float(rr[0]) + __uint_as_float(rr[1]);
    }
    LDS_AS char* xme = lds + wid * 16896;
    LDS_AS char* xpt = lds + (wid ^ 2) * 16896;
    LDS_AS char* ot = lds + 4 * 16896 + wid * (32 * 272);
    if (kh == 0) epi_send<0>(xme, lane, iseq<16>{}); else epi_send<64>(xme, lane, iseq<16>{});
    *(LDS_AS float*)(xme + 16384 + lane * 4) = m_run;
    *(LDS_AS float*)(xme + 16640 + lane * 4) = l_run;
    __syncthreads();
    {
      const float m2 = *(LDS_AS float*)(xpt + 16384 + lane * 4), l2 = *(LDS_AS float*)(xpt + 16640 + lane * 4);
      const float mn = fmaxf(m_run, m2);
      float a1 = __builtin_amdgcn_exp2f(m_run - mn), a2 = __builtin_amdgcn_exp2f(m2 - mn);
      const float inv = 1.0f / (l_run * a1 + l2 * a2);
      a1 *= inv; a2 *= inv;
      if (kh == 0) epi_comb<0>(xpt, ot, lane, a1, a2, iseq<16>{}); else epi_comb<64>(xpt, ot, lane, a1, a2, iseq<16>{});
      u16* op = ctx + (size_t)(q0 + (lane >> 4)) * HDALL + h * HD + kh * 128 + (lane & 15) * 8;
#pragma unroll
      for (int it = 0; it < 8; ++it) {
        u32x4 v = *(LDS_AS u32x4*)(ot + (it * 4 + (lane >> 4)) * 272 + (lane & 15) * 16);
        *reinterpret_cast<u32x4*>(op + (size_t)it * 4 * HDALL) = v;
      }
    }
  }
}

__device__ __forceinline__ void glds16(const void* gsrc, unsigned lds_dst) {
  unsigned keep;
  asm volatile("s_mov_b32 %0, m0\n\ts_mov_b32 m0, %2\n\ts_nop 0\n\tglobal_load_lds_dwordx4 %1, off\n\ts_mov_b32 m0, %0"
               : "=&s"(keep) : "v"(gsrc), "s"(lds_dst) : "memory");
}

constexpr int FNST = 8, FSTG = 16384, FIN_SHM = FNST * FSTG;
constexpr int FK1 = HDALL / 64, FK2 = FF / 64, FKT = FK1 + FK2;

__global__ __launch_bounds__(256) void final_kernel(const u16* __restrict__ ctx, const u16* __restrict__ wo, const u16* __restrict__ act,
                                                    const u16* __restrict__ w2, const u16* __restrict__ x, u16* __restrict__ out) {
  extern __shared__ __attribute__((aligned(16))) char shm[];
  const int b = blockIdx.x, xi = b >> 3;
  const int pm = (b & 7) * 4 + xi / 7, pn = xi % 7;
  const int brow = pm * 64, bcol = pn * 64, tid = threadIdx.x;
  const int wid = tid >> 6, lane = tid & 63, wr = wid >> 1, wc = wid & 1, fr = lane & 15, fq = lane >> 4;
  const int sr0 = tid >> 3, spc = tid & 7;
  const int ac0 = spc ^ ((sr0 >> 1) & 7);
  const int bf0 = ((sr0 >> 1) & 1) | (((sr0 >> 3) & 1) << 1);
  const int bc0 = spc ^ (bf0 << 1);
  const unsigned lds_base = (unsigned)(uintptr_t)(LDS_AS char*)shm;
  const int wid_s = __builtin_amdgcn_readfirstlane(wid);
  auto stage = [&](int kt) {
    const int kc = kt < FKT ? kt : FKT - 1;
    const bool s1 = kc < FK1;
    const u16* Ap = s1 ? ctx + kc * 64 : act + (kc - FK1) * 64;
    const int lda = s1 ? HDALL : FF;
    const u16* Bp = s1 ? wo + (size_t)kc * 64 * MD : w2 + (size_t)(kc - FK1) * 64 * MD;
    const unsigned dst = lds_base + (kt % FNST) * FSTG + wid_s * 1024;
    glds16(Ap + (size_t)(brow + sr0) * lda + ac0 * 8, dst);
    glds16(Ap + (size_t)(brow + sr0 + 32) * lda + ac0 * 8, dst + 4096);
    glds16(Bp + (size_t)sr0 * MD + bcol + bc0 * 8, dst + 8192);
    glds16(Bp + (size_t)(sr0 + 32) * MD + bcol + bc0 * 8, dst + 12288);
  };
  const int bq = (lane >> 2) & 3, bp = lane & 3;
  int aoff[2], boff[2];
#pragma unroll
  for (int m = 0; m < 2; ++m) { int r = wr * 32 + m * 16 + fr; aoff[m] = r * 128 + ((fq ^ ((r >> 1) & 7)) << 4); }
#pragma unroll
  for (int n = 0; n < 2; ++n) {
    int r = fq * 8 + bq, f = ((r >> 1) & 1) | (((r >> 3) & 1) << 1), cp = wc * 2 + n;
    boff[n] = 8192 + r * 128 + (((cp * 2 + (bp >> 1)) ^ (f << 1)) << 4) + (bp & 1) * 8;
  }
  f32x4 acc[2][2] = {}, accs[2][2];
  bf16x8 afA[2][2], bfA[2][2], afB[2][2], bfB[2][2];
  auto rdfrag = [&](int kt, bf16x8 (&af)[2][2], bf16x8 (&bf)[2][2]) {
    LDS_AS char* sb = (LDS_AS char*)shm + (kt % FNST) * FSTG;
#pragma unroll
    for (int kk = 0; kk < 2; ++kk) {
#pragma unroll
      for (int m = 0; m < 2; ++m) af[kk][m] = *(LDS_AS bf16x8*)(sb + (aoff[m] ^ (kk * 64)));
#pragma unroll
      for (int n = 0; n < 2; ++n) {
        LDS_AS char* p = sb + boff[n] + kk * 4096;
        s16x4 lo = __builtin_amdgcn_ds_read_tr16_b64_v4i16((LDS_AS s16x4*)p);
        s16x4 hh = __builtin_amdgcn_ds_read_tr16_b64_v4i16((LDS_AS s16x4*)(p + 512));
        bf[kk][n] = (bf16x8){lo[0], lo[1], lo[2], lo[3], hh[0], hh[1], hh[2], hh[3]};
      }
    }
  };
  auto mma = [&](bf16x8 (&af)[2][2], bf16x8 (&bf)[2][2]) {
#pragma unroll
    for (int kk = 0; kk < 2; ++kk)
#pragma unroll
      for (int m = 0; m < 2; ++m)
#pragma unroll
        for (int n = 0; n < 2; ++n) acc[m][n] = __builtin_amdgcn_mfma_f32_16x16x32_bf16(bf[kk][n], af[kk][m], acc[m][n], 0, 0, 0);
  };
  auto two_steps = [&](int kt) {
    asm volatile("s_waitcnt vmcnt(20)" ::: "memory");
    __builtin_amdgcn_s_barrier();
    rdfrag(kt + 1, afB, bfB);
    stage(kt + FNST - 1);
    __builtin_amdgcn_sched_barrier(0);
    mma(afA, bfA);
    __builtin_amdgcn_sched_barrier(0);
    asm volatile("s_waitcnt vmcnt(20)" ::: "memory");
    __builtin_amdgcn_s_barrier();
    rdfrag(kt + 2, afA, bfA);
    stage(kt + FNST);
    __builtin_amdgcn_sched_barrier(0);
    mma(afB, bfB);
    __builtin_amdgcn_sched_barrier(0);
  };
#pragma unroll
  for (int t = 0; t < FNST - 1; ++t) stage(t);
  asm volatile("s_waitcnt vmcnt(24)" ::: "memory");
  __builtin_amdgcn_s_barrier();
  rdfrag(0, afA, bfA);
  for (int kt = 0; kt < FK1; kt += 2) two_steps(kt);
#pragma unroll
  for (int m = 0; m < 2; ++m)
#pragma unroll
    for (int n = 0; n < 2; ++n) { accs[m][n] = acc[m][n]; acc[m][n] = (f32x4){0.f, 0.f, 0.f, 0.f}; }
  for (int kt = FK1; kt < FKT; kt += 2) two_steps(kt);
  asm volatile("s_waitcnt vmcnt(0)" ::: "memory");
#pragma unroll
  for (int m = 0; m < 2; ++m)
#pragma unroll
    for (int n = 0; n < 2; ++n) {
      int row = brow + wr * 32 + m * 16 + fr, col = bcol + wc * 32 + n * 16 + fq * 4;
      s16x4 xv = *reinterpret_cast<const s16x4*>(x + (size_t)row * MD + col);
      float r[4];
#pragma unroll
      for (int j = 0; j < 4; ++j) {
        float s = bfr(accs[m][n][j]);
        float y = bfr(acc[m][n][j]);
        float g = bfr(y / (1.0f + expf(-y)));
        r[j] = bf2f((u16)xv[j]) + bfr(s * g);
      }
      u32x2 w = {pk2(r[0], r[1]), pk2(r[2], r[3])};
      *reinterpret_cast<u32x2*>(out + (size_t)row * MD + col) = w;
    }
}

extern "C" void kernel_launch(void* const* d_in, const int* in_sizes, int n_in, void* d_out, int out_size, void* d_ws, size_t ws_size,
                              hipStream_t stream) {
  const u16* x = (const u16*)d_in[0];
  const u16* ckv = (const u16*)d_in[1];
  const u16* wq = (const u16*)d_in[2];
  const u16* wuk = (const u16*)d_in[3];
  const u16* wuv = (const u16*)d_in[4];
  const u16* wo = (const u16*)d_in[5];
  const u16* w1 = (const u16*)d_in[7];
  const u16* b1 = (const u16*)d_in[8];
  const u16* w2 = (const u16*)d_in[9];
  char* ws = (char*)d_ws;
  u16* xn = (u16*)(ws + WS_XN); u16* Q = (u16*)(ws + WS_Q); u16* K = (u16*)(ws + WS_K); u16* Vt = (u16*)(ws + WS_VT);
  u16* act = (u16*)(ws + WS_ACT); u16* ctx = (u16*)(ws + WS_CTX);
  hipFuncSetAttribute((const void*)proj_gemm_kernel, hipFuncAttributeMaxDynamicSharedMemorySize, GEMM_SHM);
  hipFuncSetAttribute((const void*)attn_kernel, hipFuncAttributeMaxDynamicSharedMemorySize, ATT_SHM);
  ln_kernel<<<SQ / 4, 256, 0, stream>>>(x, xn);
  proj_gemm_kernel<<<N_GEMM_TILES, 512, GEMM_SHM, stream>>>(ckv, xn, wq, wuk, wuv, w1, b1, Q, K, Vt, act);
  attn_kernel<<<256, 256, ATT_SHM, stream>>>(Q, K, Vt, ctx);
  hipFuncSetAttribute((const void*)final_kernel, hipFuncAttributeMaxDynamicSharedMemorySize, FIN_SHM);
  final_kernel<<<(SQ / 64) * 7, 256, FIN_SHM, stream>>>(ctx, wo, act, w2, x, (u16*)d_out);
}
```

```cpp
#include <hip/hip_runtime.h>
#include <hip/hip_bf16.h>
#include <stdint.h>
#include <utility>

typedef short bf16x8 __attribute__((ext_vector_type(8)));
typedef short s16x4 __attribute__((ext_vector_type(4)));
typedef float f32x4 __attribute__((ext_vector_type(4)));
typedef float f32x16 __attribute__((ext_vector_type(16)));
typedef unsigned u32x2 __attribute__((ext_vector_type(2)));
typedef unsigned u32x4 __attribute__((ext_vector_type(4)));
typedef unsigned short u16;
#define LDS_AS __attribute__((address_space(3)))

constexpr int SQ = 2048, MD = 448, MDP = 512, DC = 1024, NH = 16, HD = 256, HDALL = NH * HD, FF = 1792;

constexpr size_t WS_XN = 0;
constexpr size_t WS_Q = WS_XN + (size_t)SQ * MDP * 2;
constexpr size_t WS_K = WS_Q + (size_t)NH * SQ * HD * 2;
constexpr size_t WS_VT = WS_K + (size_t)NH * SQ * HD * 2;
constexpr size_t WS_ACT = WS_VT + (size_t)NH * SQ * HD * 2;
constexpr size_t WS_CTX = WS_ACT + (size_t)SQ * FF * 2;
constexpr size_t WS_SLAB = WS_CTX + (size_t)SQ * HDALL * 2;
constexpr size_t WS_CNT = WS_SLAB + (size_t)256 * 4 * (32 * 64 + 64) * 16;
constexpr size_t WS_END = WS_CNT + 512;

__device__ __forceinline__ float bf2f(u16 v) { return __uint_as_float(((unsigned)v) << 16); }
__device__ __forceinline__ u16 f2bf(float f) { __hip_bfloat16 b = __float2bfloat16(f); return *reinterpret_cast<u16*>(&b); }
__device__ __forceinline__ float bfr(float f) { return bf2f(f2bf(f)); }
__device__ __forceinline__ unsigned pk2(float lo, float hi) {
  unsigned r; asm("v_cvt_pk_bf16_f32 %0, %1, %2" : "=v"(r) : "v"(lo), "v"(hi)); return r;
}


__device__ __forceinline__ float mish_ref(float h) {
  const float L2E = 1.4426950408889634f, LN2 = 0.6931471805599453f;
  const float e = bfr(__builtin_amdgcn_exp2f(-fabsf(h) * L2E));
  const float lbig = __builtin_amdgcn_logf(1.0f + e) * LN2;
  const float lsml = e * fmaf(-e, fmaf(-e, 0.33333334f, 0.5f), 1.0f);
  const float l = bfr(e < 0.015625f ? lsml : lbig);
  const float sp = bfr(l + fmaxf(h, 0.f));
  const float w = __builtin_amdgcn_exp2f(sp * (-2.0f * L2E));
  const float tbig = (1.0f - w) * __builtin_amdgcn_rcpf(1.0f + w);
  const float tsml = sp * fmaf(sp * sp, -0.33333334f, 1.0f);
  const float th = bfr(sp < 0.03125f ? tsml : tbig);
  return h * th;
}

__global__ __launch_bounds__(256) void ln_kernel(const u16* __restrict__ x, u16* __restrict__ xn) {
  const int row = blockIdx.x * 4 + (threadIdx.x >> 6), lane = threadIdx.x & 63;
  float v[8];
  const bool act = lane < 56;
  if (act) {
    bf16x8 r = *reinterpret_cast<const bf16x8*>(x + (size_t)row * MD + lane * 8);
#pragma unroll
    for (int j = 0; j < 8; ++j) v[j] = bf2f((u16)r[j]);
  } else {
#pragma unroll
    for (int j = 0; j < 8; ++j) v[j] = 0.f;
  }
  float s = 0.f;
#pragma unroll
  for (int j = 0; j < 8; ++j) s += v[j];
#pragma unroll
  for (int o = 32; o >= 1; o >>= 1) s += __shfl_xor(s, o);
  const float mean = s * (1.0f / MD);
  float q = 0.f;
  if (act) {
#pragma unroll
    for (int j = 0; j < 8; ++j) { float d = v[j] - mean; q += d * d; }
  }
#pragma unroll
  for (int o = 32; o >= 1; o >>= 1) q += __shfl_xor(q, o);
  const float mb = bfr(mean), vb = bfr(q * (1.0f / MD));
  const float den = bfr(sqrtf(bfr(vb + 1e-5f)));
  bf16x8 o8;
#pragma unroll
  for (int j = 0; j < 8; ++j) o8[j] = act ? (short)f2bf(bfr(v[j] - mb) / den) : (short)0;
  *reinterpret_cast<bf16x8*>(xn + (size_t)row * MDP + lane * 8) = o8;
}

constexpr int BM = 256, BK = 64, HALF = 128, HT = HALF * BK, GEMM_SHM = 8 * HT * 2;

__device__ __forceinline__ int lds_byte(int r, int c) {
  int st = (r >> 4) * 2 + (c >> 5), rr = r & 15, cc = c & 31, ob = rr * 64 + cc * 2;
  return st * 1024 + (ob ^ (((ob >> 9) & 1) << 5));
}
__device__ __forceinline__ void stage_rc(int b, int& R, int& C) {
  int st = b / 1024, sb = b % 1024, swz = sb ^ (((sb >> 9) & 1) << 5);
  R = (st >> 1) * 16 + swz / 64; C = (st & 1) * 32 + (swz % 64) / 2;
}

enum { EPI_ROWMAJOR = 0, EPI_VT = 1, EPI_MISH = 2 };

template <int EPI>
__device__ __forceinline__ void gemm_tile(const u16* __restrict__ A, int lda, const u16* __restrict__ Bg, int ldb, int kmax, int nt,
                                          int brow, int bcol, u16* __restrict__ out, int ldo, const u16* __restrict__ bias, char* shm_raw) {
  u16* shm = (u16*)shm_raw;
#define SA(b, h) (shm + ((b) * 2 + (h)) * HT)
#define SB(b, h) (shm + (4 + (b) * 2 + (h)) * HT)
#define STAGE_A(T, P, br, kt) do { const u16* _g = A + (long)(br) * lda + (long)(kt) * BK;                     \
    for (int _i = 0; _i < 2; ++_i) { int _b = (T) * 16 + _i * 8192; int _r, _c; stage_rc(_b, _r, _c);          \
      __builtin_amdgcn_global_load_lds((const unsigned*)(_g + (long)_r * lda + _c), (LDS_AS unsigned*)((char*)(P) + _b), 16, 0, 0); } } while (0)
#define STAGE_B(T, P, bc, kt) do {                                                                              \
    for (int _i = 0; _i < 2; ++_i) { int _b = (T) * 16 + _i * 8192; int _k = _b >> 8, _pc = (_b >> 4) & 15;     \
      int _c = _pc ^ (((_k & 3) | (((_k >> 3) & 1) << 2)) << 1); int _kr = (kt) * BK + _k; _kr = _kr < kmax ? _kr : kmax - 1; \
      __builtin_amdgcn_global_load_lds((const unsigned*)(Bg + (long)_kr * ldb + (bc) + _c * 8), (LDS_AS unsigned*)((char*)(P) + _b), 16, 0, 0); } } while (0)
#define LDA(dst, b, h) for (int m = 0; m < 4; ++m) for (int k = 0; k < 2; ++k) \
    dst[m][k] = *reinterpret_cast<const bf16x8*>((char*)SA(b, h) + lds_byte(wr * 64 + m * 16 + fr, k * 32 + fq * 8))
#define LDB(dst, b, h) for (int n = 0; n < 2; ++n) for (int k = 0; k < 2; ++k) {                                \
    LDS_AS char* _p = (LDS_AS char*)SB(b, h) + bbase + n * 32 + k * (32 * 256);                                 \
    _p = (LDS_AS char*)((unsigned)(uintptr_t)(LDS_AS char*)SB(b, h) + ((bbase + n * 32) ^ bxor) + k * (32 * 256)); \
    s16x4 _lo = __builtin_amdgcn_ds_read_tr16_b64_v4i16((LDS_AS s16x4*)_p);                                     \
    s16x4 _hi = __builtin_amdgcn_ds_read_tr16_b64_v4i16((LDS_AS s16x4*)(_p + 1024));                            \
    dst[n][k] = (bf16x8){_lo[0], _lo[1], _lo[2], _lo[3], _hi[0], _hi[1], _hi[2], _hi[3]}; }
#define MMA(ai, bj, At, Bt) do { __builtin_amdgcn_s_setprio(1);                                                 \
    for (int m = 0; m < 4; ++m) for (int n = 0; n < 2; ++n) for (int k = 0; k < 2; ++k) {                         \
      if constexpr (EPI == EPI_VT) acc[ai][bj][m][n] = __builtin_amdgcn_mfma_f32_16x16x32_bf16(At[m][k], Bt[n][k], acc[ai][bj][m][n], 0, 0, 0); \
      else acc[ai][bj][m][n] = __builtin_amdgcn_mfma_f32_16x16x32_bf16(Bt[n][k], At[m][k], acc[ai][bj][m][n], 0, 0, 0); }                     \
    __builtin_amdgcn_s_setprio(0); } while (0)
#define WAIT_V(n) asm volatile("s_waitcnt vmcnt(" #n ")" ::: "memory")
#define WAIT_L(n) asm volatile("s_waitcnt lgkmcnt(" #n ")" ::: "memory")
#define BAR __builtin_amdgcn_s_barrier()
#define SCHED __builtin_amdgcn_sched_barrier(0)
  int tid = threadIdx.x;
  int wid = tid >> 6, lane = tid & 63, wr = wid >> 2, wc = wid & 3, fr = lane & 15, fq = lane >> 4;
  int bq = (lane >> 2) & 3, bp = lane & 3;
  int bbase = (fq * 8 + bq) * 256 + (wc * 4 + (bp >> 1)) * 16 + (bp & 1) * 8;
  int bxor = (bq | ((fq & 1) << 2)) << 5;
  f32x4 acc[2][2][4][2] = {};
  bf16x8 At[4][2], B0[2][2], B1[2][2];
  STAGE_B(tid, SB(0, 0), bcol, 0); STAGE_A(tid, SA(0, 0), brow, 0);
  STAGE_B(tid, SB(0, 1), bcol + HALF, 0); STAGE_A(tid, SA(0, 1), brow + HALF, 0);
  if (wr == 1) BAR;
  WAIT_V(4); BAR;
  STAGE_B(tid, SB(1, 0), bcol, 1); STAGE_A(tid, SA(1, 0), brow, 1); STAGE_B(tid, SB(1, 1), bcol + HALF, 1);
  WAIT_V(6); BAR;
  for (int t = 0; t < nt - 2; t += 2) {
    LDB(B0, 0, 0); SCHED; LDA(At, 0, 0); STAGE_A(tid, SA(1, 1), brow + HALF, t + 1);
    WAIT_L(8); BAR; WAIT_L(0); SCHED; MMA(0, 0, At, B0); BAR; SCHED;
    LDB(B1, 0, 1); STAGE_B(tid, SB(0, 0), bcol, t + 2);
    BAR; WAIT_L(0); SCHED; MMA(0, 1, At, B1); BAR;
    LDA(At, 0, 1); STAGE_A(tid, SA(0, 0), brow, t + 2);
    BAR; WAIT_L(0); SCHED; MMA(1, 0, At, B0); BAR; SCHED;
    STAGE_B(tid, SB(0, 1), bcol + HALF, t + 2);
    WAIT_V(6); BAR; MMA(1, 1, At, B1); BAR;
    LDB(B0, 1, 0); SCHED; LDA(At, 1, 0); STAGE_A(tid, SA(0, 1), brow + HALF, t + 2);
    WAIT_L(8); BAR; WAIT_L(0); SCHED; MMA(0, 0, At, B0); BAR; SCHED;
    LDB(B1, 1, 1); STAGE_B(tid, SB(1, 0), bcol, t + 3);
    BAR; WAIT_L(0); SCHED; MMA(0, 1, At, B1); BAR;
    LDA(At, 1, 1); STAGE_A(tid, SA(1, 0), brow, t + 3);
    BAR; WAIT_L(0); SCHED; MMA(1, 0, At, B0); BAR; SCHED;
    STAGE_B(tid, SB(1, 1), bcol + HALF, t + 3);
    WAIT_V(6); BAR; MMA(1, 1, At, B1); BAR;
  }
  int tz = threadIdx.x; asm volatile("" : "+v"(tz));
  wid = tz >> 6; lane = tz & 63; wr = wid >> 2; wc = wid & 3; fr = lane & 15; fq = lane >> 4;
  bq = (lane >> 2) & 3; bp = lane & 3;
  bbase = (fq * 8 + bq) * 256 + (wc * 4 + (bp >> 1)) * 16 + (bp & 1) * 8;
  bxor = (bq | ((fq & 1) << 2)) << 5;
  { LDB(B0, 0, 0); WAIT_V(0); LDA(At, 0, 0); STAGE_A(tz, SA(1, 1), brow + HALF, nt - 1);
    BAR; WAIT_L(0); SCHED; MMA(0, 0, At, B0); BAR;
    LDB(B1, 0, 1); BAR; WAIT_L(0); SCHED; MMA(0, 1, At, B1); BAR;
    LDA(At, 0, 1); WAIT_V(4); BAR; WAIT_L(0); SCHED; MMA(1, 0, At, B0); MMA(1, 1, At, B1); BAR; }
  { LDB(B0, 1, 0); LDA(At, 1, 0); WAIT_V(2); BAR; WAIT_L(0); SCHED; MMA(0, 0, At, B0); BAR;
    LDB(B1, 1, 1); WAIT_V(0); BAR; WAIT_L(0); SCHED; MMA(0, 1, At, B1); BAR;
    LDA(At, 1, 1); BAR; WAIT_L(0); SCHED; MMA(1, 0, At, B0); MMA(1, 1, At, B1); BAR; }
  if (wr == 0) BAR;
#pragma unroll
  for (int ai = 0; ai < 2; ++ai)
#pragma unroll
    for (int bj = 0; bj < 2; ++bj)
#pragma unroll
      for (int m = 0; m < 4; ++m)
#pragma unroll
        for (int n = 0; n < 2; ++n) {
          f32x4 a = acc[ai][bj][m][n];
          if constexpr (EPI == EPI_VT) {
            int d = bj * HALF + wc * 32 + n * 16 + fr;
            int key0 = brow + ai * HALF + wr * 64 + m * 16 + (((fq & 1) << 1) | (fq >> 1)) * 4;
            u32x2 w = {pk2(a[0], a[1]), pk2(a[2], a[3])};
            *reinterpret_cast<u32x2*>(out + (size_t)(key0 >> 6) * (HD * 64) + d * 64 + (key0 & 63)) = w;
          } else {
            int row = brow + ai * HALF + wr * 64 + m * 16 + fr;
            int col = bj * HALF + wc * 32 + n * 16 + fq * 4;
            if constexpr (EPI == EPI_MISH) {
              s16x4 bb = *reinterpret_cast<const s16x4*>(bias + bcol + col);
              float r[4];
#pragma unroll
              for (int j = 0; j < 4; ++j) r[j] = bfr(a[j]) + bf2f((u16)bb[j]);
              u32x2 w = {pk2(r[0], r[1]), pk2(r[2], r[3])};
              *reinterpret_cast<u32x2*>(out + (size_t)row * ldo + bcol + col) = w;
            } else {
              u32x2 w = {pk2(a[0], a[1]), pk2(a[2], a[3])};
              *reinterpret_cast<u32x2*>(out + (size_t)row * ldo + col) = w;
            }
          }
        }
#undef SA
#undef SB
#undef STAGE_A
#undef STAGE_B
#undef LDA
#undef LDB
#undef MMA
}

constexpr int N_KV_TILES = 256, N_Q_TILES = 128, N_UP_TILES = 56, N_GEMM_TILES = N_KV_TILES + N_Q_TILES + N_UP_TILES;

__global__ __launch_bounds__(512, 2) void proj_gemm_kernel(const u16* __restrict__ ckv, const u16* __restrict__ xn, const u16* __restrict__ wq,
                                                           const u16* __restrict__ wuk, const u16* __restrict__ wuv, const u16* __restrict__ w1,
                                                           const u16* __restrict__ b1, u16* __restrict__ Qo, u16* __restrict__ Ko,
                                                           u16* __restrict__ Vto, u16* __restrict__ acto, unsigned* __restrict__ cnt) {
  extern __shared__ __attribute__((aligned(16))) char shm[];
  const int b = blockIdx.x;
  if (b == 0 && threadIdx.x < 128) cnt[threadIdx.x] = 0u;
  if (b < N_KV_TILES) {
    const int xcd = b & 7, i = b >> 3;
    const int pn = xcd * 4 + (i & 3), pm = i >> 2;
    if (pn < 16) gemm_tile<EPI_ROWMAJOR>(ckv, DC, wuk, HDALL, DC, DC / BK, pm * BM, pn * BM, Ko + (size_t)pn * SQ * HD, HD, nullptr, shm);
    else gemm_tile<EPI_VT>(ckv, DC, wuv, HDALL, DC, DC / BK, pm * BM, (pn - 16) * BM, Vto + (size_t)(pn - 16) * SQ * HD, SQ, nullptr, shm);
  } else if (b < N_KV_TILES + N_Q_TILES) {
    const int j = b - N_KV_TILES, xcd = j & 7, i = j >> 3;
    const int pn = xcd * 2 + (i & 1), pm = i >> 1;
    gemm_tile<EPI_ROWMAJOR>(xn, MDP, wq, HDALL, MD, MDP / BK, pm * BM, pn * BM, Qo + (size_t)pn * SQ * HD, HD, nullptr, shm);
  } else {
    const int j = b - N_KV_TILES - N_Q_TILES, pm = j & 7, pn = j >> 3;
    gemm_tile<EPI_MISH>(xn, MDP, w1, FF, MD, MDP / BK, pm * BM, pn * BM, acto, FF, b1, shm);
  }
}

constexpr int KT = 64;
constexpr int KBUF_B = KT * HD * 2;
constexpr int ATT_SHM = 4 * KBUF_B;
constexpr float SM_C = 0.0625f * 1.4426950408889634f;
constexpr float RESC_THR = 8.0f;
constexpr int SPLIT_T = 17;

typedef __amdgpu_buffer_rsrc_t rsrc_t;
__device__ __forceinline__ rsrc_t mk_rsrc(const void* p, unsigned bytes) {
  return __builtin_amdgcn_make_buffer_rsrc((void*)p, 0, bytes, 0x00020000);
}
template <int N> using iseq = std::make_integer_sequence<int, N>;
#define SB0() __builtin_amdgcn_sched_barrier(0)

template <int I> __device__ __forceinline__ void acc_zero1() { asm volatile("v_accvgpr_write_b32 a[%c0], 0" :: "i"(I)); }
template <int... Is> __device__ __forceinline__ void acc_zero(std::integer_sequence<int, Is...>) { (acc_zero1<Is>(), ...); }
template <int I> __device__ __forceinline__ void acc_scale1(float alpha) {
  float tmp;
  asm volatile("v_accvgpr_read_b32 %0, a[%c2]\n\tv_mul_f32 %0, %0, %1\n\tv_accvgpr_write_b32 a[%c2], %0" : "=&v"(tmp) : "v"(alpha), "i"(I));
}
template <int... Is> __device__ __forceinline__ void acc_scale(float alpha, std::integer_sequence<int, Is...>) { (acc_scale1<Is>(alpha), ...); }
template <int I> __device__ __forceinline__ float acc_read() { float x; asm volatile("v_accvgpr_read_b32 %0, a[%c1]" : "=v"(x) : "i"(I)); return x; }
template <int R> __device__ __forceinline__ f32x4 acc_read4() { return (f32x4){acc_read<R>(), acc_read<R + 1>(), acc_read<R + 2>(), acc_read<R + 3>()}; }
template <int S> __device__ __forceinline__ void q_load1(rsrc_t r, unsigned voff, unsigned soff) {
  asm volatile("buffer_load_dwordx4 a[%c3:%c4], %0, %1, %2 offen" :: "v"(voff), "s"(r), "s"(soff + S * 32), "i"(128 + 4 * S), "i"(131 + 4 * S) : "memory");
}
template <int... Ss> __device__ __forceinline__ void q_load(rsrc_t r, unsigned voff, unsigned soff, std::integer_sequence<int, Ss...>) { (q_load1<Ss>(r, voff, soff), ...); }
template <int J> __device__ __forceinline__ void stage_step(unsigned waddr, rsrc_t r, unsigned voff, unsigned soff) {
  constexpr int OFF = J < 8 ? (J >> 1) * 8192 : (J - 8) * 4096;
  asm volatile("s_waitcnt vmcnt(15)\n\tds_write_b128 %0, a[%c1:%c2] offset:%c3\n\tbuffer_load_dwordx4 a[%c1:%c2], %4, %5, %6 offen"
               :: "v"(waddr), "i"(192 + 4 * J), "i"(195 + 4 * J), "i"(OFF), "v"(voff), "s"(r), "s"(soff) : "memory");
}
template <int J> __device__ __forceinline__ void stage_load(rsrc_t r, unsigned voff, unsigned soff) {
  asm volatile("buffer_load_dwordx4 a[%c0:%c1], %2, %3, %4 offen" :: "i"(192 + 4 * J), "i"(195 + 4 * J), "v"(voff), "s"(r), "s"(soff) : "memory");
}
template <int J> __device__ __forceinline__ void stage_write(unsigned waddr) {
  constexpr int OFF = J < 8 ? (J >> 1) * 8192 : (J - 8) * 4096;
  asm volatile("ds_write_b128 %0, a[%c1:%c2] offset:%c3" :: "v"(waddr), "i"(192 + 4 * J), "i"(195 + 4 * J), "i"(OFF) : "memory");
}
template <int J0, int... Js> __device__ __forceinline__ void stage_load8(rsrc_t r, unsigned voff, unsigned soff, std::integer_sequence<int, Js...>) {
  (stage_load<J0 + Js>(r, voff, soff + Js * 4096), ...);
}

constexpr int LEAD = 6;
struct AttnRegs {
  f32x16 sA[2], sB[2], cin[2];
  bf16x8 fr[8];
  unsigned pw[16];
  float pe[2];
  float ps, m_run, mx0, mx1;
  unsigned kra[8], vra[4];
  unsigned kw0, kw1, vw, kg, vg;
};
template <int F> __device__ __forceinline__ void frag_read(AttnRegs& c) {
  if constexpr (F < 32) {
    asm volatile("ds_read_b128 %0, %1 offset:%c2" : "=v"(c.fr[F & 7]) : "v"(c.kra[F & 7]), "i"((F >> 4) * 16384 + ((F >> 3) & 1) * 256) : "memory");
  } else {
    asm volatile("ds_read_b128 %0, %1 offset:%c2" : "=v"(c.fr[F & 7]) : "v"(c.vra[(F - 32) >> 3]), "i"(((F - 32) & 7) * 4096) : "memory");
  }
}
template <int G> __device__ __forceinline__ void gap_mfma(AttnRegs& c) {
  constexpr int W = (63 - G) < (LEAD - 1) ? (63 - G) : (LEAD - 1);
  if constexpr (G < 32) {
    constexpr int U = G >> 4, S = G & 15;
    if constexpr (S == 0)
      asm volatile("s_waitcnt lgkmcnt(%c3)\n\tv_mfma_f32_32x32x16_bf16 %0, %1, a[128:131], %2" : "=&v"(c.sB[U]) : "v"(c.fr[G & 7]), "v"(c.cin[U]), "i"(W));
    else
      asm volatile("s_waitcnt lgkmcnt(%c4)\n\tv_mfma_f32_32x32x16_bf16 %0, %1, a[%c2:%c3], %0" : "+v"(c.sB[U]) : "v"(c.fr[G & 7]), "i"(128 + 4 * S), "i"(131 + 4 * S), "i"(W));
  } else {
    constexpr int KS = (G - 32) >> 3, D = (G - 32) & 7;
    u32x4 pv = {c.pw[4 * KS], c.pw[4 * KS + 1], c.pw[4 * KS + 2], c.pw[4 * KS + 3]};
    asm volatile("s_waitcnt lgkmcnt(%c4)\n\ts_nop 1\n\tv_mfma_f32_32x32x16_bf16 a[%c2:%c3], %0, %1, a[%c2:%c3]" :: "v"(c.fr[G & 7]), "v"(pv), "i"(D * 16), "i"(D * 16 + 15), "i"(W));
  }
}
template <int E> __device__ __forceinline__ void sm_elem(AttnRegs& c) {
  const float p = __builtin_amdgcn_exp2f(fmaf(c.sA[E >> 4][E & 15], SM_C, -c.m_run));
  c.ps += p;
  if constexpr (E & 1) c.pw[E >> 1] = pk2(c.pe[0], p); else c.pe[0] = p;
}
constexpr int elem_gap(int e) { return (e * 17) / 10; }
template <int G, int E> __device__ __forceinline__ void sm_for_gap(AttnRegs& c) {
  if constexpr (E < 32) {
    if constexpr (elem_gap(E) == G) sm_elem<E>(c);
    if constexpr (elem_gap(E) <= G) sm_for_gap<G, E + 1>(c);
  }
}
template <int G> __device__ __forceinline__ void gap(AttnRegs& c, rsrc_t rK, rsrc_t rV, unsigned ks, unsigned vs) {
  gap_mfma<G>(c);
  if constexpr (G + LEAD < 64) frag_read<G + LEAD>(c);
  sm_for_gap<G, 0>(c);
  if constexpr (G < 32 && (G & 3) == 2) { constexpr int J = G >> 2; stage_step<J>((J & 1) ? c.kw1 : c.kw0, rK, c.kg, ks + J * 4096); }
  if constexpr (G >= 32 && (G & 3) == 2) { constexpr int J = (G - 32) >> 2; stage_step<8 + J>(c.vw, rV, c.vg, vs + J * 4096); }
  if constexpr (G == 35) { asm volatile("" : "+v"(c.sB[0]), "+v"(c.sB[1])); c.mx0 = fmaxf(c.sB[0][0], c.sB[0][1]); c.mx1 = fmaxf(c.sB[1][0], c.sB[1][1]); }
  if constexpr (G >= 36 && G < 43) { constexpr int R = 2 + 2 * (G - 36); c.mx0 = fmaxf(fmaxf(c.mx0, c.sB[0][R]), c.sB[0][R + 1]); }
  if constexpr (G >= 43 && G < 50) { constexpr int R = 2 + 2 * (G - 43); c.mx1 = fmaxf(fmaxf(c.mx1, c.sB[1][R]), c.sB[1][R + 1]); }
  SB0();
}
template <int... Gs> __device__ __forceinline__ void gaps(AttnRegs& c, rsrc_t rK, rsrc_t rV, unsigned ks, unsigned vs, std::integer_sequence<int, Gs...>) {
  (gap<Gs>(c, rK, rV, ks, vs), ...);
}
template <int... Fs> __device__ __forceinline__ void frag_reads(AttnRegs& c, std::integer_sequence<int, Fs...>) { (frag_read<Fs>(c), ...); }
__device__ __forceinline__ float xhalf_max(float v) {
  auto rr = __builtin_amdgcn_permlane32_swap(__float_as_uint(v), __float_as_uint(v), false, false);
  return fmaxf(__uint_as_float(rr[0]), __uint_as_float(rr[1]));
}
template <bool MERGE, int G> __device__ __forceinline__ void epi1(LDS_AS char* ot, const f32x4* __restrict__ slab, int lane, float a1, float a2) {
  f32x4 o1 = acc_read4<4 * G>();
  if constexpr (MERGE) {
    const f32x4 o2 = slab[G * 64 + lane];
    o1[0] = o1[0] * a1 + o2[0] * a2; o1[1] = o1[1] * a1 + o2[1] * a2; o1[2] = o1[2] * a1 + o2[2] * a2; o1[3] = o1[3] * a1 + o2[3] * a2;
  } else { o1[0] *= a1; o1[1] *= a1; o1[2] *= a1; o1[3] *= a1; }
  u32x2 w = {pk2(o1[0], o1[1]), pk2(o1[2], o1[3])};
  *(LDS_AS u32x2*)(ot + (lane & 31) * 528 + ((G >> 2) * 32 + 8 * (G & 3) + 4 * (lane >> 5)) * 2) = w;
}
template <bool MERGE, int... Gs> __device__ __forceinline__ void epi_all(LDS_AS char* ot, const f32x4* __restrict__ slab, int lane, float a1, float a2, std::integer_sequence<int, Gs...>) {
  (epi1<MERGE, Gs>(ot, slab, lane, a1, a2), ...);
}
template <int... Gs> __device__ __forceinline__ void slab_store(f32x4* __restrict__ slab, int lane, std::integer_sequence<int, Gs...>) {
  ((slab[Gs * 64 + lane] = acc_read4<4 * Gs>()), ...);
}

constexpr size_t SLAB_F4 = 4 * (32 * 64 + 64);

__global__ __launch_bounds__(256, 1) void attn_kernel(const u16* __restrict__ Q, const u16* __restrict__ K, const u16* __restrict__ Vt,
                                                      u16* __restrict__ ctx, u16* __restrict__ act, f32x4* __restrict__ slabs,
                                                      unsigned* __restrict__ cnt) {
  extern __shared__ __attribute__((aligned(16))) char shm[];
  const int b = blockIdx.x, xcd = b & 7, ii = b >> 3;
  const int h = xcd * 2 + (ii & 1), x = (ii >> 1) & 7, role = ii >> 4;
  const rsrc_t rK = mk_rsrc(K + (size_t)h * SQ * HD, SQ * HD * 2);
  const rsrc_t rV = mk_rsrc(Vt + (size_t)h * HD * SQ, SQ * HD * 2);
  const rsrc_t rQ = mk_rsrc(Q + (size_t)h * SQ * HD, SQ * HD * 2);
  LDS_AS char* lds = (LDS_AS char*)shm;
  const unsigned lds0 = (unsigned)(uintptr_t)lds;
  constexpr int VB0 = 2 * KBUF_B;
  asm volatile("" ::: "a0", "a63", "a127", "a128", "a191", "a255");
  const int nseg = role == 0 ? 1 : 2;

  for (int seg = 0; seg < nseg; ++seg) {
    int tid = threadIdx.x; asm volatile("" : "+v"(tid));
    const int wid = __builtin_amdgcn_readfirstlane(tid >> 6), lane = tid & 63, r32 = lane & 31, hi = lane >> 5;
    const bool partial = seg == 0;
    const int qb = partial ? 15 - x : x;
    const int t0 = (partial && role == 1) ? SPLIT_T : 0;
    const int t1 = partial ? (role == 0 ? SPLIT_T : 2 * qb + 2) : 2 * qb + 2;
    const int last = t1 - 1;
    const int q0 = qb * 128 + wid * 32;
    AttnRegs c;
    c.m_run = -1e30f;
    float l_run = 0.f;
    c.kg = (tid >> 5) * 512 + (tid & 31) * 16;
    c.vg = (tid >> 3) * 128 + (tid & 7) * 16;
    c.kw0 = lds0 + (tid >> 5) * 512 + (((tid & 31) ^ (tid >> 5)) << 4);
    c.kw1 = lds0 + ((tid >> 5) + 8) * 512 + (((tid & 31) ^ ((tid >> 5) + 8)) << 4);
    c.vw = lds0 + VB0 + (tid >> 3) * 128 + (((tid & 7) ^ ((tid >> 4) & 7)) << 4);
#pragma unroll
    for (int s = 0; s < 8; ++s) c.kra[s] = lds0 + r32 * 512 + (((2 * s + hi) ^ (r32 & 15)) << 4);
#pragma unroll
    for (int k4 = 0; k4 < 4; ++k4) c.vra[k4] = lds0 + VB0 + r32 * 128 + (((2 * k4 + hi) ^ ((r32 >> 1) & 7)) << 4);
    auto set_mask = [&](int t) {
      const int lim = t > last ? -1000 : q0 + r32 - 64 * t - 4 * hi;
#pragma unroll
      for (int u = 0; u < 2; ++u)
#pragma unroll
        for (int r = 0; r < 16; ++r) c.cin[u][r] = (32 * u + (r & 3) + 8 * (r >> 2)) > lim ? -INFINITY : 0.f;
    };
    auto tclamp = [&](int t) { return (unsigned)(t < last ? t : last) * KBUF_B; };

    __syncthreads();
    stage_load8<0>(rK, c.kg, t0 * KBUF_B, iseq<8>{});
    stage_load8<8>(rV, c.vg, t0 * KBUF_B, iseq<8>{});
    q_load(rQ, r32 * 512 + hi * 16, q0 * 512, iseq<16>{});
    acc_zero(iseq<128>{});
    if (seg == 0) {
      u16* ap = act + (size_t)blockIdx.x * (SQ * FF / 256) + tid * 8;
      bf16x8 hv[7];
#pragma unroll
      for (int i = 0; i < 7; ++i) hv[i] = *reinterpret_cast<const bf16x8*>(ap + i * 2048);
#pragma unroll
      for (int i = 0; i < 7; ++i) {
        u32x4 w;
#pragma unroll
        for (int j = 0; j < 4; ++j) w[j] = pk2(mish_ref(bf2f((u16)hv[i][2 * j])), mish_ref(bf2f((u16)hv[i][2 * j + 1])));
        *reinterpret_cast<u32x4*>(ap + i * 2048) = w;
      }
    }
    asm volatile("s_waitcnt vmcnt(16)" ::: "memory");
    stage_write<0>(c.kw0); stage_write<1>(c.kw1); stage_write<2>(c.kw0); stage_write<3>(c.kw1);
    stage_write<4>(c.kw0); stage_write<5>(c.kw1); stage_write<6>(c.kw0); stage_write<7>(c.kw1);
    stage_write<8>(c.vw); stage_write<9>(c.vw); stage_write<10>(c.vw); stage_write<11>(c.vw);
    stage_write<12>(c.vw); stage_write<13>(c.vw); stage_write<14>(c.vw); stage_write<15>(c.vw);
    stage_load8<0>(rK, c.kg, tclamp(t0 + 1), iseq<8>{});
    asm volatile("s_waitcnt vmcnt(0)" ::: "memory");
    { const unsigned a0 = c.kw0 + KBUF_B, a1 = c.kw1 + KBUF_B;
      stage_write<0>(a0); stage_write<1>(a1); stage_write<2>(a0); stage_write<3>(a1);
      stage_write<4>(a0); stage_write<5>(a1); stage_write<6>(a0); stage_write<7>(a1); }
    stage_load8<0>(rK, c.kg, tclamp(t0 + 2), iseq<8>{});
    stage_load8<8>(rV, c.vg, tclamp(t0 + 1), iseq<8>{});
    asm volatile("s_waitcnt lgkmcnt(0)" ::: "memory");
    __builtin_amdgcn_s_barrier();
    set_mask(t0);
#pragma unroll
    for (int u = 0; u < 2; ++u) {
      f32x16 acc = c.cin[u];
#pragma unroll
      for (int s = 0; s < 16; ++s) {
        bf16x8 kf = *(LDS_AS bf16x8*)(uintptr_t)(c.kra[s & 7] + u * 16384 + (s >> 3) * 256);
        if (s == 0) asm volatile("s_nop 1" ::: "memory");
        switch (s) {
#define QS(n) case n: asm volatile("v_mfma_f32_32x32x16_bf16 %0, %1, a[%c2:%c3], %0" : "+v"(acc) : "v"(kf), "i"(128 + 4 * n), "i"(131 + 4 * n)); break;
          QS(0) QS(1) QS(2) QS(3) QS(4) QS(5) QS(6) QS(7) QS(8) QS(9) QS(10) QS(11) QS(12) QS(13) QS(14) QS(15)
#undef QS
        }
      }
      asm volatile("s_nop 15\n\ts_nop 15" : "+v"(acc));
      c.sA[u] = acc;
    }
    {
      float pm = c.sA[0][0];
#pragma unroll
      for (int r = 1; r < 16; ++r) pm = fmaxf(pm, c.sA[0][r]);
#pragma unroll
      for (int r = 0; r < 16; ++r) pm = fmaxf(pm, c.sA[1][r]);
      c.m_run = fmaxf(c.m_run, xhalf_max(pm) * SM_C);
    }
    set_mask(t0 + 1);
#pragma unroll
    for (int s = 0; s < 8; ++s) c.kra[s] ^= KBUF_B;
    c.vw ^= KBUF_B;
    SB0();
    __builtin_amdgcn_s_barrier();
    for (int t = t0; t < t1; ++t) {
      const unsigned ks = tclamp(t + 3), vs = tclamp(t + 2);
      c.ps = 0.f;
      frag_reads(c, iseq<LEAD>{});
      SB0();
      gaps(c, rK, rV, ks, vs, iseq<64>{});
      l_run += c.ps;
      const float pmax = xhalf_max(fmaxf(c.mx0, c.mx1)) * SM_C;
      if (!__all(pmax - c.m_run <= RESC_THR)) {
        const float mn = fmaxf(c.m_run, pmax);
        const float alpha = __builtin_amdgcn_exp2f(c.m_run - mn);
        c.m_run = mn;
        l_run *= alpha;
        asm volatile("s_nop 15\n\ts_nop 15" ::: "memory");
        acc_scale(alpha, iseq<128>{});
        asm volatile("s_nop 3" ::: "memory");
      }
      if (t + 2 >= 2 * qb || t + 2 > last) set_mask(t + 2);
      c.sA[0] = c.sB[0]; c.sA[1] = c.sB[1];
#pragma unroll
      for (int s = 0; s < 8; ++s) c.kra[s] ^= KBUF_B;
#pragma unroll
      for (int k4 = 0; k4 < 4; ++k4) c.vra[k4] ^= KBUF_B;
      c.kw0 ^= KBUF_B; c.kw1 ^= KBUF_B; c.vw ^= KBUF_B;
      SB0();
      asm volatile("s_waitcnt lgkmcnt(0)" ::: "memory");
      __builtin_amdgcn_s_barrier();
    }
    asm volatile("s_waitcnt vmcnt(0)\n\ts_nop 15\n\ts_nop 15" ::: "memory");
    {
      auto rr = __builtin_amdgcn_permlane32_swap(__float_as_uint(l_run), __float_as_uint(l_run), false, false);
      l_run = __uint_as_float(rr[0]) + __uint_as_float(rr[1]);
    }
    LDS_AS char* ot = lds + wid * (32 * 528);
    bool do_out = true;
    float a1, a2 = 0.f;
    const f32x4* oslab = nullptr;
    if (partial) {
      f32x4* mys = slabs + ((size_t)(h * 8 + x) * 2 + role) * SLAB_F4 + wid * (SLAB_F4 / 4);
      slab_store(mys, lane, iseq<32>{});
      mys[32 * 64 + lane] = (f32x4){c.m_run, l_run, 0.f, 0.f};
      asm volatile("s_waitcnt vmcnt(0)" ::: "memory");
      __syncthreads();
      LDS_AS unsigned* flag = (LDS_AS unsigned*)(lds + 4 * 32 * 528);
      if (tid == 0) {
        __builtin_amdgcn_fence(__ATOMIC_RELEASE, "agent");
        asm volatile("s_waitcnt vmcnt(0)" ::: "memory");
        const unsigned old = __hip_atomic_fetch_add(cnt + h * 8 + x, 1u, __ATOMIC_RELAXED, __HIP_MEMORY_SCOPE_AGENT);
        if (old == 1u) {
          __builtin_amdgcn_fence(__ATOMIC_ACQUIRE, "agent");
          asm volatile("s_waitcnt vmcnt(0)" ::: "memory");
        }
        *flag = old;
      }
      __syncthreads();
      do_out = (*flag == 1u);
      if (do_out) {
        oslab = slabs + ((size_t)(h * 8 + x) * 2 + (role ^ 1)) * SLAB_F4 + wid * (SLAB_F4 / 4);
        const f32x4 ml = oslab[32 * 64 + lane];
        const float mn = fmaxf(c.m_run, ml[0]);
        a1 = __builtin_amdgcn_exp2f(c.m_run - mn); a2 = __builtin_amdgcn_exp2f(ml[0] - mn);
        const float inv = 1.0f / (l_run * a1 + ml[1] * a2);
        a1 *= inv; a2 *= inv;
      }
    } else {
      a1 = 1.0f / l_run;
    }
    if (do_out) {
      if (partial) epi_all<true>(ot, oslab, lane, a1, a2, iseq<32>{});
      else epi_all<false>(ot, oslab, lane, a1, a2, iseq<32>{});
      u16* op = ctx + (size_t)(q0 + (lane >> 5)) * HDALL + h * HD + (lane & 31) * 8;
#pragma unroll
      for (int it = 0; it < 16; ++it) {
        u32x4 v = *(LDS_AS u32x4*)(ot + (it * 2 + (lane >> 5)) * 528 + (lane & 31) * 16);
        *reinterpret_cast<u32x4*>(op + (size_t)it * 2 * HDALL) = v;
      }
    }
  }
}

__device__ __forceinline__ void glds16(const void* gsrc, unsigned lds_dst) {
  unsigned keep;
  asm volatile("s_mov_b32 %0, m0\n\ts_mov_b32 m0, %2\n\ts_nop 0\n\tglobal_load_lds_dwordx4 %1, off\n\ts_mov_b32 m0, %0"
               : "=&s"(keep) : "v"(gsrc), "s"(lds_dst) : "memory");
}

constexpr int FNST = 8, FSTG = 16384, FIN_SHM = FNST * FSTG;
constexpr int FK1 = HDALL / 64, FK2 = FF / 64, FKT = FK1 + FK2;

__global__ __launch_bounds__(256) void final_kernel(const u16* __restrict__ ctx, const u16* __restrict__ wo, const u16* __restrict__ act,
                                                    const u16* __restrict__ w2, const u16* __restrict__ x, u16* __restrict__ out) {
  extern __shared__ __attribute__((aligned(16))) char shm[];
  const int b = blockIdx.x, xi = b >> 3;
  const int pm = (b & 7) * 4 + xi / 7, pn = xi % 7;
  const int brow = pm * 64, bcol = pn * 64, tid = threadIdx.x;
  const int wid = tid >> 6, lane = tid & 63, wr = wid >> 1, wc = wid & 1, fr = lane & 15, fq = lane >> 4;
  const int sr0 = tid >> 3, spc = tid & 7;
  const int ac0 = spc ^ ((sr0 >> 1) & 7);
  const int bf0 = ((sr0 >> 1) & 1) | (((sr0 >> 3) & 1) << 1);
  const int bc0 = spc ^ (bf0 << 1);
  const unsigned lds_base = (unsigned)(uintptr_t)(LDS_AS char*)shm;
  const int wid_s = __builtin_amdgcn_readfirstlane(wid);
  auto stage = [&](int kt) {
    const int kc = kt < FKT ? kt : FKT - 1;
    const bool s1 = kc < FK1;
    const u16* Ap = s1 ? ctx + kc * 64 : act + (kc - FK1) * 64;
    const int lda = s1 ? HDALL : FF;
    const u16* Bp = s1 ? wo + (size_t)kc * 64 * MD : w2 + (size_t)(kc - FK1) * 64 * MD;
    const unsigned dst = lds_base + (kt % FNST) * FSTG + wid_s * 1024;
    glds16(Ap + (size_t)(brow + sr0) * lda + ac0 * 8, dst);
    glds16(Ap + (size_t)(brow + sr0 + 32) * lda + ac0 * 8, dst + 4096);
    glds16(Bp + (size_t)sr0 * MD + bcol + bc0 * 8, dst + 8192);
    glds16(Bp + (size_t)(sr0 + 32) * MD + bcol + bc0 * 8, dst + 12288);
  };
  const int bq = (lane >> 2) & 3, bp = lane & 3;
  int aoff[2], boff[2];
#pragma unroll
  for (int m = 0; m < 2; ++m) { int r = wr * 32 + m * 16 + fr; aoff[m] = r * 128 + ((fq ^ ((r >> 1) & 7)) << 4); }
#pragma unroll
  for (int n = 0; n < 2; ++n) {
    int r = fq * 8 + bq, f = ((r >> 1) & 1) | (((r >> 3) & 1) << 1), cp = wc * 2 + n;
    boff[n] = 8192 + r * 128 + (((cp * 2 + (bp >> 1)) ^ (f << 1)) << 4) + (bp & 1) * 8;
  }
  f32x4 acc[2][2] = {}, accs[2][2];
  bf16x8 afA[2][2], bfA[2][2], afB[2][2], bfB[2][2];
  auto rdfrag = [&](int kt, bf16x8 (&af)[2][2], bf16x8 (&bf)[2][2]) {
    LDS_AS char* sb = (LDS_AS char*)shm + (kt % FNST) * FSTG;
#pragma unroll
    for (int kk = 0; kk < 2; ++kk) {
#pragma unroll
      for (int m = 0; m < 2; ++m) af[kk][m] = *(LDS_AS bf16x8*)(sb + (aoff[m] ^ (kk * 64)));
#pragma unroll
      for (int n = 0; n < 2; ++n) {
        LDS_AS char* p = sb + boff[n] + kk * 4096;
        s16x4 lo = __builtin_amdgcn_ds_read_tr16_b64_v4i16((LDS_AS s16x4*)p);
        s16x4 hh = __builtin_amdgcn_ds_read_tr16_b64_v4i16((LDS_AS s16x4*)(p + 512));
        bf[kk][n] = (bf16x8){lo[0], lo[1], lo[2], lo[3], hh[0], hh[1], hh[2], hh[3]};
      }
    }
  };
  auto mma = [&](bf16x8 (&af)[2][2], bf16x8 (&bf)[2][2]) {
#pragma unroll
    for (int kk = 0; kk < 2; ++kk)
#pragma unroll
      for (int m = 0; m < 2; ++m)
#pragma unroll
        for (int n = 0; n < 2; ++n) acc[m][n] = __builtin_amdgcn_mfma_f32_16x16x32_bf16(bf[kk][n], af[kk][m], acc[m][n], 0, 0, 0);
  };
  auto two_steps = [&](int kt) {
    asm volatile("s_waitcnt vmcnt(20)" ::: "memory");
    __builtin_amdgcn_s_barrier();
    rdfrag(kt + 1, afB, bfB);
    stage(kt + FNST - 1);
    __builtin_amdgcn_sched_barrier(0);
    mma(afA, bfA);
    __builtin_amdgcn_sched_barrier(0);
    asm volatile("s_waitcnt vmcnt(20)" ::: "memory");
    __builtin_amdgcn_s_barrier();
    rdfrag(kt + 2, afA, bfA);
    stage(kt + FNST);
    __builtin_amdgcn_sched_barrier(0);
    mma(afB, bfB);
    __builtin_amdgcn_sched_barrier(0);
  };
#pragma unroll
  for (int t = 0; t < FNST - 1; ++t) stage(t);
  asm volatile("s_waitcnt vmcnt(24)" ::: "memory");
  __builtin_amdgcn_s_barrier();
  rdfrag(0, afA, bfA);
  for (int kt = 0; kt < FK1; kt += 2) two_steps(kt);
#pragma unroll
  for (int m = 0; m < 2; ++m)
#pragma unroll
    for (int n = 0; n < 2; ++n) { accs[m][n] = acc[m][n]; acc[m][n] = (f32x4){0.f, 0.f, 0.f, 0.f}; }
  for (int kt = FK1; kt < FKT; kt += 2) two_steps(kt);
  asm volatile("s_waitcnt vmcnt(0)" ::: "memory");
#pragma unroll
  for (int m = 0; m < 2; ++m)
#pragma unroll
    for (int n = 0; n < 2; ++n) {
      int row = brow + wr * 32 + m * 16 + fr, col = bcol + wc * 32 + n * 16 + fq * 4;
      s16x4 xv = *reinterpret_cast<const s16x4*>(x + (size_t)row * MD + col);
      float r[4];
#pragma unroll
      for (int j = 0; j < 4; ++j) {
        float s = bfr(accs[m][n][j]);
        float y = bfr(acc[m][n][j]);
        float g = bfr(y / (1.0f + expf(-y)));
        r[j] = bf2f((u16)xv[j]) + bfr(s * g);
      }
      u32x2 w = {pk2(r[0], r[1]), pk2(r[2], r[3])};
      *reinterpret_cast<u32x2*>(out + (size_t)row * MD + col) = w;
    }
}

extern "C" void kernel_launch(void* const* d_in, const int* in_sizes, int n_in, void* d_out, int out_size, void* d_ws, size_t ws_size,
                              hipStream_t stream) {
  const u16* x = (const u16*)d_in[0];
  const u16* ckv = (const u16*)d_in[1];
  const u16* wq = (const u16*)d_in[2];
  const u16* wuk = (const u16*)d_in[3];
  const u16* wuv = (const u16*)d_in[4];
  const u16* wo = (const u16*)d_in[5];
  const u16* w1 = (const u16*)d_in[7];
  const u16* b1 = (const u16*)d_in[8];
  const u16* w2 = (const u16*)d_in[9];
  char* ws = (char*)d_ws;
  u16* xn = (u16*)(ws + WS_XN); u16* Q = (u16*)(ws + WS_Q); u16* K = (u16*)(ws + WS_K); u16* Vt = (u16*)(ws + WS_VT);
  u16* act = (u16*)(ws + WS_ACT); u16* ctx = (u16*)(ws + WS_CTX);
  hipFuncSetAttribute((const void*)proj_gemm_kernel, hipFuncAttributeMaxDynamicSharedMemorySize, GEMM_SHM);
  hipFuncSetAttribute((const void*)attn_kernel, hipFuncAttributeMaxDynamicSharedMemorySize, ATT_SHM);
  ln_kernel<<<SQ / 4, 256, 0, stream>>>(x, xn);
  proj_gemm_kernel<<<N_GEMM_TILES, 512, GEMM_SHM, stream>>>(ckv, xn, wq, wuk, wuv, w1, b1, Q, K, Vt, act, (unsigned*)(ws + WS_CNT));
  attn_kernel<<<256, 256, ATT_SHM, stream>>>(Q, K, Vt, ctx, act, (f32x4*)(ws + WS_SLAB), (unsigned*)(ws + WS_CNT));
  hipFuncSetAttribute((const void*)final_kernel, hipFuncAttributeMaxDynamicSharedMemorySize, FIN_SHM);
  final_kernel<<<(SQ / 64) * 7, 256, FIN_SHM, stream>>>(ctx, wo, act, w2, x, (u16*)d_out);
}
```

```cpp
#include <hip/hip_runtime.h>
#include <hip/hip_bf16.h>
#include <stdint.h>
#include <utility>

typedef short bf16x8 __attribute__((ext_vector_type(8)));
typedef short s16x4 __attribute__((ext_vector_type(4)));
typedef float f32x4 __attribute__((ext_vector_type(4)));
typedef float f32x16 __attribute__((ext_vector_type(16)));
typedef unsigned u32x2 __attribute__((ext_vector_type(2)));
typedef unsigned u32x4 __attribute__((ext_vector_type(4)));
typedef unsigned short u16;
#define LDS_AS __attribute__((address_space(3)))

constexpr int SQ = 2048, MD = 448, MDP = 512, DC = 1024, NH = 16, HD = 256, HDALL = NH * HD, FF = 1792;

constexpr size_t WS_XN = 0;
constexpr size_t WS_Q = WS_XN + (size_t)SQ * MDP * 2;
constexpr size_t WS_K = WS_Q + (size_t)NH * SQ * HD * 2;
constexpr size_t WS_VT = WS_K + (size_t)NH * SQ * HD * 2;
constexpr size_t WS_ACT = WS_VT + (size_t)NH * SQ * HD * 2;
constexpr size_t WS_CTX = WS_ACT + (size_t)SQ * FF * 2;
constexpr size_t WS_SLAB = WS_CTX + (size_t)SQ * HDALL * 2;
constexpr size_t WS_CNT = WS_SLAB + (size_t)256 * 4 * (32 * 64 + 64) * 16;
constexpr size_t WS_LUT = WS_CNT + 512;
constexpr size_t WS_END = WS_LUT + 65536 * 2;

__device__ __forceinline__ float bf2f(u16 v) { return __uint_as_float(((unsigned)v) << 16); }
__device__ __forceinline__ u16 f2bf(float f) { __hip_bfloat16 b = __float2bfloat16(f); return *reinterpret_cast<u16*>(&b); }
__device__ __forceinline__ float bfr(float f) { return bf2f(f2bf(f)); }
__device__ __forceinline__ unsigned pk2(float lo, float hi) {
  unsigned r; asm("v_cvt_pk_bf16_f32 %0, %1, %2" : "=v"(r) : "v"(lo), "v"(hi)); return r;
}


__device__ __forceinline__ float mish_ref(float h) {
  const float L2E = 1.4426950408889634f, LN2 = 0.6931471805599453f;
  const float e = bfr(__builtin_amdgcn_exp2f(-fabsf(h) * L2E));
  const float lbig = __builtin_amdgcn_logf(1.0f + e) * LN2;
  const float lsml = e * fmaf(-e, fmaf(-e, 0.33333334f, 0.5f), 1.0f);
  const float l = bfr(e < 0.015625f ? lsml : lbig);
  const float sp = bfr(l + fmaxf(h, 0.f));
  const float w = __builtin_amdgcn_exp2f(sp * (-2.0f * L2E));
  const float tbig = (1.0f - w) * __builtin_amdgcn_rcpf(1.0f + w);
  const float tsml = sp * fmaf(sp * sp, -0.33333334f, 1.0f);
  const float th = bfr(sp < 0.03125f ? tsml : tbig);
  return h * th;
}

__global__ __launch_bounds__(256) void ln_kernel(const u16* __restrict__ x, u16* __restrict__ xn, u16* __restrict__ lut) {
  const int row = blockIdx.x * 4 + (threadIdx.x >> 6), lane = threadIdx.x & 63;
  { const int gid = blockIdx.x * 256 + threadIdx.x; if (gid < 65536) lut[gid] = f2bf(mish_ref(bf2f((u16)gid))); }
  float v[8];
  const bool act = lane < 56;
  if (act) {
    bf16x8 r = *reinterpret_cast<const bf16x8*>(x + (size_t)row * MD + lane * 8);
#pragma unroll
    for (int j = 0; j < 8; ++j) v[j] = bf2f((u16)r[j]);
  } else {
#pragma unroll
    for (int j = 0; j < 8; ++j) v[j] = 0.f;
  }
  float s = 0.f;
#pragma unroll
  for (int j = 0; j < 8; ++j) s += v[j];
#pragma unroll
  for (int o = 32; o >= 1; o >>= 1) s += __shfl_xor(s, o);
  const float mean = s * (1.0f / MD);
  float q = 0.f;
  if (act) {
#pragma unroll
    for (int j = 0; j < 8; ++j) { float d = v[j] - mean; q += d * d; }
  }
#pragma unroll
  for (int o = 32; o >= 1; o >>= 1) q += __shfl_xor(q, o);
  const float mb = bfr(mean), vb = bfr(q * (1.0f / MD));
  const float den = bfr(sqrtf(bfr(vb + 1e-5f)));
  bf16x8 o8;
#pragma unroll
  for (int j = 0; j < 8; ++j) o8[j] = act ? (short)f2bf(bfr(v[j] - mb) / den) : (short)0;
  *reinterpret_cast<bf16x8*>(xn + (size_t)row * MDP + lane * 8) = o8;
}

constexpr int BM = 256, BK = 64, HALF = 128, HT = HALF * BK, GEMM_SHM = 8 * HT * 2;

__device__ __forceinline__ int lds_byte(int r, int c) {
  int st = (r >> 4) * 2 + (c >> 5), rr = r & 15, cc = c & 31, ob = rr * 64 + cc * 2;
  return st * 1024 + (ob ^ (((ob >> 9) & 1) << 5));
}
__device__ __forceinline__ void stage_rc(int b, int& R, int& C) {
  int st = b / 1024, sb = b % 1024, swz = sb ^ (((sb >> 9) & 1) << 5);
  R = (st >> 1) * 16 + swz / 64; C = (st & 1) * 32 + (swz % 64) / 2;
}

enum { EPI_ROWMAJOR = 0, EPI_VT = 1, EPI_MISH = 2 };

template <int EPI>
__device__ __forceinline__ void gemm_tile(const u16* __restrict__ A, int lda, const u16* __restrict__ Bg, int ldb, int kmax, int nt,
                                          int brow, int bcol, u16* __restrict__ out, int ldo, const u16* __restrict__ bias, char* shm_raw) {
  u16* shm = (u16*)shm_raw;
#define SA(b, h) (shm + ((b) * 2 + (h)) * HT)
#define SB(b, h) (shm + (4 + (b) * 2 + (h)) * HT)
#define STAGE_A(T, P, br, kt) do { const u16* _g = A + (long)(br) * lda + (long)(kt) * BK;                     \
    for (int _i = 0; _i < 2; ++_i) { int _b = (T) * 16 + _i * 8192; int _r, _c; stage_rc(_b, _r, _c);          \
      __builtin_amdgcn_global_load_lds((const unsigned*)(_g + (long)_r * lda + _c), (LDS_AS unsigned*)((char*)(P) + _b), 16, 0, 0); } } while (0)
#define STAGE_B(T, P, bc, kt) do {                                                                              \
    for (int _i = 0; _i < 2; ++_i) { int _b = (T) * 16 + _i * 8192; int _k = _b >> 8, _pc = (_b >> 4) & 15;     \
      int _c = _pc ^ (((_k & 3) | (((_k >> 3) & 1) << 2)) << 1); int _kr = (kt) * BK + _k; _kr = _kr < kmax ? _kr : kmax - 1; \
      __builtin_amdgcn_global_load_lds((const unsigned*)(Bg + (long)_kr * ldb + (bc) + _c * 8), (LDS_AS unsigned*)((char*)(P) + _b), 16, 0, 0); } } while (0)
#define LDA(dst, b, h) for (int m = 0; m < 4; ++m) for (int k = 0; k < 2; ++k) \
    dst[m][k] = *reinterpret_cast<const bf16x8*>((char*)SA(b, h) + lds_byte(wr * 64 + m * 16 + fr, k * 32 + fq * 8))
#define LDB(dst, b, h) for (int n = 0; n < 2; ++n) for (int k = 0; k < 2; ++k) {                                \
    LDS_AS char* _p = (LDS_AS char*)SB(b, h) + bbase + n * 32 + k * (32 * 256);                                 \
    _p = (LDS_AS char*)((unsigned)(uintptr_t)(LDS_AS char*)SB(b, h) + ((bbase + n * 32) ^ bxor) + k * (32 * 256)); \
    s16x4 _lo = __builtin_amdgcn_ds_read_tr16_b64_v4i16((LDS_AS s16x4*)_p);                                     \
    s16x4 _hi = __builtin_amdgcn_ds_read_tr16_b64_v4i16((LDS_AS s16x4*)(_p + 1024));                            \
    dst[n][k] = (bf16x8){_lo[0], _lo[1], _lo[2], _lo[3], _hi[0], _hi[1], _hi[2], _hi[3]}; }
#define MMA(ai, bj, At, Bt) do { __builtin_amdgcn_s_setprio(1);                                                 \
    for (int m = 0; m < 4; ++m) for (int n = 0; n < 2; ++n) for (int k = 0; k < 2; ++k) {                         \
      if constexpr (EPI == EPI_VT) acc[ai][bj][m][n] = __builtin_amdgcn_mfma_f32_16x16x32_bf16(At[m][k], Bt[n][k], acc[ai][bj][m][n], 0, 0, 0); \
      else acc[ai][bj][m][n] = __builtin_amdgcn_mfma_f32_16x16x32_bf16(Bt[n][k], At[m][k], acc[ai][bj][m][n], 0, 0, 0); }                     \
    __builtin_amdgcn_s_setprio(0); } while (0)
#define WAIT_V(n) asm volatile("s_waitcnt vmcnt(" #n ")" ::: "memory")
#define WAIT_L(n) asm volatile("s_waitcnt lgkmcnt(" #n ")" ::: "memory")
#define BAR __builtin_amdgcn_s_barrier()
#define SCHED __builtin_amdgcn_sched_barrier(0)
  int tid = threadIdx.x;
  int wid = tid >> 6, lane = tid & 63, wr = wid >> 2, wc = wid & 3, fr = lane & 15, fq = lane >> 4;
  int bq = (lane >> 2) & 3, bp = lane & 3;
  int bbase = (fq * 8 + bq) * 256 + (wc * 4 + (bp >> 1)) * 16 + (bp & 1) * 8;
  int bxor = (bq | ((fq & 1) << 2)) << 5;
  f32x4 acc[2][2][4][2] = {};
  bf16x8 At[4][2], B0[2][2], B1[2][2];
  STAGE_B(tid, SB(0, 0), bcol, 0); STAGE_A(tid, SA(0, 0), brow, 0);
  STAGE_B(tid, SB(0, 1), bcol + HALF, 0); STAGE_A(tid, SA(0, 1), brow + HALF, 0);
  if (wr == 1) BAR;
  WAIT_V(4); BAR;
  STAGE_B(tid, SB(1, 0), bcol, 1); STAGE_A(tid, SA(1, 0), brow, 1); STAGE_B(tid, SB(1, 1), bcol + HALF, 1);
  WAIT_V(6); BAR;
  for (int t = 0; t < nt - 2; t += 2) {
    LDB(B0, 0, 0); SCHED; LDA(At, 0, 0); STAGE_A(tid, SA(1, 1), brow + HALF, t + 1);
    WAIT_L(8); BAR; WAIT_L(0); SCHED; MMA(0, 0, At, B0); BAR; SCHED;
    LDB(B1, 0, 1); STAGE_B(tid, SB(0, 0), bcol, t + 2);
    BAR; WAIT_L(0); SCHED; MMA(0, 1, At, B1); BAR;
    LDA(At, 0, 1); STAGE_A(tid, SA(0, 0), brow, t + 2);
    BAR; WAIT_L(0); SCHED; MMA(1, 0, At, B0); BAR; SCHED;
    STAGE_B(tid, SB(0, 1), bcol + HALF, t + 2);
    WAIT_V(6); BAR; MMA(1, 1, At, B1); BAR;
    LDB(B0, 1, 0); SCHED; LDA(At, 1, 0); STAGE_A(tid, SA(0, 1), brow + HALF, t + 2);
    WAIT_L(8); BAR; WAIT_L(0); SCHED; MMA(0, 0, At, B0); BAR; SCHED;
    LDB(B1, 1, 1); STAGE_B(tid, SB(1, 0), bcol, t + 3);
    BAR; WAIT_L(0); SCHED; MMA(0, 1, At, B1); BAR;
    LDA(At, 1, 1); STAGE_A(tid, SA(1, 0), brow, t + 3);
    BAR; WAIT_L(0); SCHED; MMA(1, 0, At, B0); BAR; SCHED;
    STAGE_B(tid, SB(1, 1), bcol + HALF, t + 3);
    WAIT_V(6); BAR; MMA(1, 1, At, B1); BAR;
  }
  int tz = threadIdx.x; asm volatile("" : "+v"(tz));
  wid = tz >> 6; lane = tz & 63; wr = wid >> 2; wc = wid & 3; fr = lane & 15; fq = lane >> 4;
  bq = (lane >> 2) & 3; bp = lane & 3;
  bbase = (fq * 8 + bq) * 256 + (wc * 4 + (bp >> 1)) * 16 + (bp & 1) * 8;
  bxor = (bq | ((fq & 1) << 2)) << 5;
  { LDB(B0, 0, 0); WAIT_V(0); LDA(At, 0, 0); STAGE_A(tz, SA(1, 1), brow + HALF, nt - 1);
    BAR; WAIT_L(0); SCHED; MMA(0, 0, At, B0); BAR;
    LDB(B1, 0, 1); BAR; WAIT_L(0); SCHED; MMA(0, 1, At, B1); BAR;
    LDA(At, 0, 1); WAIT_V(4); BAR; WAIT_L(0); SCHED; MMA(1, 0, At, B0); MMA(1, 1, At, B1); BAR; }
  { LDB(B0, 1, 0); LDA(At, 1, 0); WAIT_V(2); BAR; WAIT_L(0); SCHED; MMA(0, 0, At, B0); BAR;
    LDB(B1, 1, 1); WAIT_V(0); BAR; WAIT_L(0); SCHED; MMA(0, 1, At, B1); BAR;
    LDA(At, 1, 1); BAR; WAIT_L(0); SCHED; MMA(1, 0, At, B0); MMA(1, 1, At, B1); BAR; }
  if (wr == 0) BAR;
#pragma unroll
  for (int ai = 0; ai < 2; ++ai)
#pragma unroll
    for (int bj = 0; bj < 2; ++bj)
#pragma unroll
      for (int m = 0; m < 4; ++m)
#pragma unroll
        for (int n = 0; n < 2; ++n) {
          f32x4 a = acc[ai][bj][m][n];
          if constexpr (EPI == EPI_VT) {
            int d = bj * HALF + wc * 32 + n * 16 + fr;
            int key0 = brow + ai * HALF + wr * 64 + m * 16 + (((fq & 1) << 1) | (fq >> 1)) * 4;
            u32x2 w = {pk2(a[0], a[1]), pk2(a[2], a[3])};
            *reinterpret_cast<u32x2*>(out + (size_t)(key0 >> 6) * (HD * 64) + d * 64 + (key0 & 63)) = w;
          } else {
            int row = brow + ai * HALF + wr * 64 + m * 16 + fr;
            int col = bj * HALF + wc * 32 + n * 16 + fq * 4;
            if constexpr (EPI == EPI_MISH) {
              s16x4 bb = *reinterpret_cast<const s16x4*>(bias + bcol + col);
              float r[4];
#pragma unroll
              for (int j = 0; j < 4; ++j) r[j] = bfr(a[j]) + bf2f((u16)bb[j]);
              u32x2 w = {pk2(r[0], r[1]), pk2(r[2], r[3])};
              *reinterpret_cast<u32x2*>(out + (size_t)row * ldo + bcol + col) = w;
            } else {
              u32x2 w = {pk2(a[0], a[1]), pk2(a[2], a[3])};
              *reinterpret_cast<u32x2*>(out + (size_t)row * ldo + col) = w;
            }
          }
        }
#undef SA
#undef SB
#undef STAGE_A
#undef STAGE_B
#undef LDA
#undef LDB
#undef MMA
}

constexpr int N_KV_TILES = 256, N_Q_TILES = 128, N_UP_TILES = 56, N_GEMM_TILES = N_KV_TILES + N_Q_TILES + N_UP_TILES;

__global__ __launch_bounds__(512, 2) void proj_gemm_kernel(const u16* __restrict__ ckv, const u16* __restrict__ xn, const u16* __restrict__ wq,
                                                           const u16* __restrict__ wuk, const u16* __restrict__ wuv, const u16* __restrict__ w1,
                                                           const u16* __restrict__ b1, u16* __restrict__ Qo, u16* __restrict__ Ko,
                                                           u16* __restrict__ Vto, u16* __restrict__ acto, unsigned* __restrict__ cnt) {
  extern __shared__ __attribute__((aligned(16))) char shm[];
  const int b = blockIdx.x;
  if (b == 0 && threadIdx.x < 128) cnt[threadIdx.x] = 0u;
  if (b < N_KV_TILES) {
    const int xcd = b & 7, i = b >> 3;
    const int pn = xcd * 4 + (i & 3), pm = i >> 2;
    if (pn < 16) gemm_tile<EPI_ROWMAJOR>(ckv, DC, wuk, HDALL, DC, DC / BK, pm * BM, pn * BM, Ko + (size_t)pn * SQ * HD, HD, nullptr, shm);
    else gemm_tile<EPI_VT>(ckv, DC, wuv, HDALL, DC, DC / BK, pm * BM, (pn - 16) * BM, Vto + (size_t)(pn - 16) * SQ * HD, SQ, nullptr, shm);
  } else if (b < N_KV_TILES + N_Q_TILES) {
    const int j = b - N_KV_TILES, xcd = j & 7, i = j >> 3;
    const int pn = xcd * 2 + (i & 1), pm = i >> 1;
    gemm_tile<EPI_ROWMAJOR>(xn, MDP, wq, HDALL, MD, MDP / BK, pm * BM, pn * BM, Qo + (size_t)pn * SQ * HD, HD, nullptr, shm);
  } else {
    const int j = b - N_KV_TILES - N_Q_TILES, pm = j & 7, pn = j >> 3;
    gemm_tile<EPI_MISH>(xn, MDP, w1, FF, MD, MDP / BK, pm * BM, pn * BM, acto, FF, b1, shm);
  }
}

constexpr int KT = 64;
constexpr int KBUF_B = KT * HD * 2;
constexpr int ATT_SHM = 4 * KBUF_B;
constexpr float SM_C = 0.0625f * 1.4426950408889634f;
constexpr float RESC_THR = 8.0f;
constexpr int SPLIT_T = 17;

typedef __amdgpu_buffer_rsrc_t rsrc_t;
__device__ __forceinline__ rsrc_t mk_rsrc(const void* p, unsigned bytes) {
  return __builtin_amdgcn_make_buffer_rsrc((void*)p, 0, bytes, 0x00020000);
}
template <int N> using iseq = std::make_integer_sequence<int, N>;
#define SB0() __builtin_amdgcn_sched_barrier(0)

template <int I> __device__ __forceinline__ void acc_zero1() { asm volatile("v_accvgpr_write_b32 a[%c0], 0" :: "i"(I)); }
template <int... Is> __device__ __forceinline__ void acc_zero(std::integer_sequence<int, Is...>) { (acc_zero1<Is>(), ...); }
template <int I> __device__ __forceinline__ void acc_scale1(float alpha) {
  float tmp;
  asm volatile("v_accvgpr_read_b32 %0, a[%c2]\n\tv_mul_f32 %0, %0, %1\n\tv_accvgpr_write_b32 a[%c2], %0" : "=&v"(tmp) : "v"(alpha), "i"(I));
}
template <int... Is> __device__ __forceinline__ void acc_scale(float alpha, std::integer_sequence<int, Is...>) { (acc_scale1<Is>(alpha), ...); }
template <int I> __device__ __forceinline__ float acc_read() { float x; asm volatile("v_accvgpr_read_b32 %0, a[%c1]" : "=v"(x) : "i"(I)); return x; }
template <int R> __device__ __forceinline__ f32x4 acc_read4() { return (f32x4){acc_read<R>(), acc_read<R + 1>(), acc_read<R + 2>(), acc_read<R + 3>()}; }
template <int S> __device__ __forceinline__ void q_load1(rsrc_t r, unsigned voff, unsigned soff) {
  asm volatile("buffer_load_dwordx4 a[%c3:%c4], %0, %1, %2 offen" :: "v"(voff), "s"(r), "s"(soff + S * 32), "i"(128 + 4 * S), "i"(131 + 4 * S) : "memory");
}
template <int... Ss> __device__ __forceinline__ void q_load(rsrc_t r, unsigned voff, unsigned soff, std::integer_sequence<int, Ss...>) { (q_load1<Ss>(r, voff, soff), ...); }
template <int J> __device__ __forceinline__ void stage_step(unsigned waddr, rsrc_t r, unsigned voff, unsigned soff) {
  constexpr int OFF = J < 8 ? (J >> 1) * 8192 : (J - 8) * 4096;
  asm volatile("s_waitcnt vmcnt(15)\n\tds_write_b128 %0, a[%c1:%c2] offset:%c3\n\tbuffer_load_dwordx4 a[%c1:%c2], %4, %5, %6 offen"
               :: "v"(waddr), "i"(192 + 4 * J), "i"(195 + 4 * J), "i"(OFF), "v"(voff), "s"(r), "s"(soff) : "memory");
}
template <int J> __device__ __forceinline__ void stage_load(rsrc_t r, unsigned voff, unsigned soff) {
  asm volatile("buffer_load_dwordx4 a[%c0:%c1], %2, %3, %4 offen" :: "i"(192 + 4 * J), "i"(195 + 4 * J), "v"(voff), "s"(r), "s"(soff) : "memory");
}
template <int J> __device__ __forceinline__ void stage_write(unsigned waddr) {
  constexpr int OFF = J < 8 ? (J >> 1) * 8192 : (J - 8) * 4096;
  asm volatile("ds_write_b128 %0, a[%c1:%c2] offset:%c3" :: "v"(waddr), "i"(192 + 4 * J), "i"(195 + 4 * J), "i"(OFF) : "memory");
}
template <int J0, int... Js> __device__ __forceinline__ void stage_load8(rsrc_t r, unsigned voff, unsigned soff, std::integer_sequence<int, Js...>) {
  (stage_load<J0 + Js>(r, voff, soff + Js * 4096), ...);
}

constexpr int LEAD = 7;
struct AttnRegs {
  f32x16 sA[2], sB[2], cin[2];
  bf16x8 fr[8];
  unsigned pw[16];
  float pe[2];
  float ps, m_run, mx0, mx1;
  unsigned kra[8], vra[4];
  unsigned kw0, kw1, vw, kg, vg;
};
template <int F> __device__ __forceinline__ void frag_read(AttnRegs& c) {
  if constexpr (F < 32) {
    asm volatile("ds_read_b128 %0, %1 offset:%c2" : "=v"(c.fr[F & 7]) : "v"(c.kra[F & 7]), "i"((F >> 4) * 16384 + ((F >> 3) & 1) * 256) : "memory");
  } else {
    asm volatile("ds_read_b128 %0, %1 offset:%c2" : "=v"(c.fr[F & 7]) : "v"(c.vra[(F - 32) >> 3]), "i"(((F - 32) & 7) * 4096) : "memory");
  }
}
template <int G> __device__ __forceinline__ void gap_mfma(AttnRegs& c) {
  constexpr int W = (62 - G) < (LEAD - 2) ? (62 - G) : (LEAD - 2);
  if constexpr ((G & 1) == 0) asm volatile("s_waitcnt lgkmcnt(%c0)" :: "i"(W) : "memory");
  if constexpr (G < 32) {
    constexpr int U = G >> 4, S = G & 15;
    if constexpr (S == 0)
      asm volatile("v_mfma_f32_32x32x16_bf16 %0, %1, a[128:131], %2" : "=&v"(c.sB[U]) : "v"(c.fr[G & 7]), "v"(c.cin[U]));
    else
      asm volatile("v_mfma_f32_32x32x16_bf16 %0, %1, a[%c2:%c3], %0" : "+v"(c.sB[U]) : "v"(c.fr[G & 7]), "i"(128 + 4 * S), "i"(131 + 4 * S));
  } else {
    constexpr int KS = (G - 32) >> 3, D = (G - 32) & 7;
    u32x4 pv = {c.pw[4 * KS], c.pw[4 * KS + 1], c.pw[4 * KS + 2], c.pw[4 * KS + 3]};
    asm volatile("v_mfma_f32_32x32x16_bf16 a[%c2:%c3], %0, %1, a[%c2:%c3]" :: "v"(c.fr[G & 7]), "v"(pv), "i"(D * 16), "i"(D * 16 + 15));
  }
}
template <int E> __device__ __forceinline__ void sm_elem(AttnRegs& c) {
  const float p = __builtin_amdgcn_exp2f(fmaf(c.sA[E >> 4][E & 15], SM_C, -c.m_run));
  c.ps += p;
  if constexpr (E & 1) c.pw[E >> 1] = pk2(c.pe[0], p); else c.pe[0] = p;
}
constexpr int elem_gap(int e) { return (e * 17) / 10; }
template <int G, int E> __device__ __forceinline__ void sm_for_gap(AttnRegs& c) {
  if constexpr (E < 32) {
    if constexpr (elem_gap(E) == G) sm_elem<E>(c);
    if constexpr (elem_gap(E) <= G) sm_for_gap<G, E + 1>(c);
  }
}
template <int G> __device__ __forceinline__ void gap(AttnRegs& c, rsrc_t rK, rsrc_t rV, unsigned ks, unsigned vs) {
  gap_mfma<G>(c);
  if constexpr (G + LEAD < 64) frag_read<G + LEAD>(c);
  sm_for_gap<G, 0>(c);
  if constexpr (G < 32 && (G & 3) == 2) { constexpr int J = G >> 2; stage_step<J>((J & 1) ? c.kw1 : c.kw0, rK, c.kg, ks + J * 4096); }
  if constexpr (G >= 32 && (G & 3) == 2) { constexpr int J = (G - 32) >> 2; stage_step<8 + J>(c.vw, rV, c.vg, vs + J * 4096); }
  if constexpr (G == 35) { asm volatile("" : "+v"(c.sB[0]), "+v"(c.sB[1])); c.mx0 = fmaxf(c.sB[0][0], c.sB[0][1]); c.mx1 = fmaxf(c.sB[1][0], c.sB[1][1]); }
  if constexpr (G >= 36 && G < 43) { constexpr int R = 2 + 2 * (G - 36); c.mx0 = fmaxf(fmaxf(c.mx0, c.sB[0][R]), c.sB[0][R + 1]); }
  if constexpr (G >= 43 && G < 50) { constexpr int R = 2 + 2 * (G - 43); c.mx1 = fmaxf(fmaxf(c.mx1, c.sB[1][R]), c.sB[1][R + 1]); }
  SB0();
}
template <int... Gs> __device__ __forceinline__ void gaps(AttnRegs& c, rsrc_t rK, rsrc_t rV, unsigned ks, unsigned vs, std::integer_sequence<int, Gs...>) {
  (gap<Gs>(c, rK, rV, ks, vs), ...);
}
template <int... Fs> __device__ __forceinline__ void frag_reads(AttnRegs& c, std::integer_sequence<int, Fs...>) { (frag_read<Fs>(c), ...); }
__device__ __forceinline__ float xhalf_max(float v) {
  auto rr = __builtin_amdgcn_permlane32_swap(__float_as_uint(v), __float_as_uint(v), false, false);
  return fmaxf(__uint_as_float(rr[0]), __uint_as_float(rr[1]));
}
template <bool MERGE, int G> __device__ __forceinline__ void epi1(LDS_AS char* ot, const u32x4 (&sl)[16], int lane, float a1, float a2) {
  f32x4 o1 = acc_read4<4 * G>();
  if constexpr (MERGE) {
    const f32x4 o2 = *reinterpret_cast<const f32x4*>(&sl[G & 15]);
    o1[0] = o1[0] * a1 + o2[0] * a2; o1[1] = o1[1] * a1 + o2[1] * a2; o1[2] = o1[2] * a1 + o2[2] * a2; o1[3] = o1[3] * a1 + o2[3] * a2;
  } else { o1[0] *= a1; o1[1] *= a1; o1[2] *= a1; o1[3] *= a1; }
  u32x2 w = {pk2(o1[0], o1[1]), pk2(o1[2], o1[3])};
  *(LDS_AS u32x2*)(ot + (lane & 31) * 528 + ((G >> 2) * 32 + 8 * (G & 3) + 4 * (lane >> 5)) * 2) = w;
}
template <bool MERGE, int G0, int... Gs> __device__ __forceinline__ void epi_all(LDS_AS char* ot, const u32x4 (&sl)[16], int lane, float a1, float a2, std::integer_sequence<int, Gs...>) {
  (epi1<MERGE, G0 + Gs>(ot, sl, lane, a1, a2), ...);
}
template <int G> __device__ __forceinline__ void slab_store1(rsrc_t r, unsigned voff) {
  const f32x4 v = acc_read4<4 * G>();
  __builtin_amdgcn_raw_buffer_store_b128(*reinterpret_cast<const u32x4*>(&v), r, voff, G * 1024, 16);
}
template <int... Gs> __device__ __forceinline__ void slab_store(rsrc_t r, unsigned voff, std::integer_sequence<int, Gs...>) { (slab_store1<Gs>(r, voff), ...); }

constexpr size_t SLAB_F4 = 4 * (32 * 64 + 64);

__global__ __launch_bounds__(256, 1) void attn_kernel(const u16* __restrict__ Q, const u16* __restrict__ K, const u16* __restrict__ Vt,
                                                      u16* __restrict__ ctx, u16* __restrict__ act, f32x4* __restrict__ slabs,
                                                      unsigned* __restrict__ cnt, const u16* __restrict__ lut) {
  extern __shared__ __attribute__((aligned(16))) char shm[];
  const int b = blockIdx.x, xcd = b & 7, ii = b >> 3;
  const int h = xcd * 2 + (ii & 1), x = (ii >> 1) & 7, role = ii >> 4;
  const rsrc_t rK = mk_rsrc(K + (size_t)h * SQ * HD, SQ * HD * 2);
  const rsrc_t rV = mk_rsrc(Vt + (size_t)h * HD * SQ, SQ * HD * 2);
  const rsrc_t rQ = mk_rsrc(Q + (size_t)h * SQ * HD, SQ * HD * 2);
  LDS_AS char* lds = (LDS_AS char*)shm;
  const unsigned lds0 = (unsigned)(uintptr_t)lds;
  constexpr int VB0 = 2 * KBUF_B;
  asm volatile("" ::: "a0", "a63", "a127", "a128", "a191", "a255");
  const int nseg = role == 0 ? 1 : 2;

  for (int seg = 0; seg < nseg; ++seg) {
    int tid = threadIdx.x; asm volatile("" : "+v"(tid));
    const int wid = __builtin_amdgcn_readfirstlane(tid >> 6), lane = tid & 63, r32 = lane & 31, hi = lane >> 5;
    const bool partial = seg == 0;
    const int qb = partial ? 15 - x : x;
    const int t0 = (partial && role == 1) ? SPLIT_T : 0;
    const int t1 = partial ? (role == 0 ? SPLIT_T : 2 * qb + 2) : 2 * qb + 2;
    const int last = t1 - 1;
    const int q0 = qb * 128 + wid * 32;
    AttnRegs c;
    c.m_run = -1e30f;
    float l_run = 0.f;
    c.kg = (tid >> 5) * 512 + (tid & 31) * 16;
    c.vg = (tid >> 3) * 128 + (tid & 7) * 16;
    c.kw0 = lds0 + (tid >> 5) * 512 + (((tid & 31) ^ (tid >> 5)) << 4);
    c.kw1 = lds0 + ((tid >> 5) + 8) * 512 + (((tid & 31) ^ ((tid >> 5) + 8)) << 4);
    c.vw = lds0 + VB0 + (tid >> 3) * 128 + (((tid & 7) ^ ((tid >> 4) & 7)) << 4);
#pragma unroll
    for (int s = 0; s < 8; ++s) c.kra[s] = lds0 + r32 * 512 + (((2 * s + hi) ^ (r32 & 15)) << 4);
#pragma unroll
    for (int k4 = 0; k4 < 4; ++k4) c.vra[k4] = lds0 + VB0 + r32 * 128 + (((2 * k4 + hi) ^ ((r32 >> 1) & 7)) << 4);
    auto set_mask = [&](int t) {
      const int lim = t > last ? -1000 : q0 + r32 - 64 * t - 4 * hi;
#pragma unroll
      for (int u = 0; u < 2; ++u)
#pragma unroll
        for (int r = 0; r < 16; ++r) c.cin[u][r] = (32 * u + (r & 3) + 8 * (r >> 2)) > lim ? -INFINITY : 0.f;
    };
    auto tclamp = [&](int t) { return (unsigned)(t < last ? t : last) * KBUF_B; };

    __syncthreads();
    stage_load8<0>(rK, c.kg, t0 * KBUF_B, iseq<8>{});
    stage_load8<8>(rV, c.vg, t0 * KBUF_B, iseq<8>{});
    q_load(rQ, r32 * 512 + hi * 16, q0 * 512, iseq<16>{});
    acc_zero(iseq<128>{});
    if (seg == 0) {
      u16* ap = act + (size_t)blockIdx.x * (SQ * FF / 256) + tid * 8;
      bf16x8 hv[7];
#pragma unroll
      for (int i = 0; i < 7; ++i) hv[i] = *reinterpret_cast<const bf16x8*>(ap + i * 2048);
#pragma unroll
      for (int i = 0; i < 7; ++i) {
        bf16x8 w;
#pragma unroll
        for (int j = 0; j < 8; ++j) w[j] = (short)lut[(u16)hv[i][j]];
        *reinterpret_cast<bf16x8*>(ap + i * 2048) = w;
      }
    }
    asm volatile("s_waitcnt vmcnt(16)" ::: "memory");
    stage_write<0>(c.kw0); stage_write<1>(c.kw1); stage_write<2>(c.kw0); stage_write<3>(c.kw1);
    stage_write<4>(c.kw0); stage_write<5>(c.kw1); stage_write<6>(c.kw0); stage_write<7>(c.kw1);
    stage_write<8>(c.vw); stage_write<9>(c.vw); stage_write<10>(c.vw); stage_write<11>(c.vw);
    stage_write<12>(c.vw); stage_write<13>(c.vw); stage_write<14>(c.vw); stage_write<15>(c.vw);
    stage_load8<0>(rK, c.kg, tclamp(t0 + 1), iseq<8>{});
    asm volatile("s_waitcnt vmcnt(0)" ::: "memory");
    { const unsigned a0 = c.kw0 + KBUF_B, a1 = c.kw1 + KBUF_B;
      stage_write<0>(a0); stage_write<1>(a1); stage_write<2>(a0); stage_write<3>(a1);
      stage_write<4>(a0); stage_write<5>(a1); stage_write<6>(a0); stage_write<7>(a1); }
    stage_load8<0>(rK, c.kg, tclamp(t0 + 2), iseq<8>{});
    stage_load8<8>(rV, c.vg, tclamp(t0 + 1), iseq<8>{});
    asm volatile("s_waitcnt lgkmcnt(0)" ::: "memory");
    __builtin_amdgcn_s_barrier();
    set_mask(t0);
#pragma unroll
    for (int u = 0; u < 2; ++u) {
      f32x16 acc = c.cin[u];
#pragma unroll
      for (int s = 0; s < 16; ++s) {
        bf16x8 kf = *(LDS_AS bf16x8*)(uintptr_t)(c.kra[s & 7] + u * 16384 + (s >> 3) * 256);
        switch (s) {
#define QS(n) case n: asm volatile("s_nop 1\n\tv_mfma_f32_32x32x16_bf16 %0, %1, a[%c2:%c3], %0" : "+v"(acc) : "v"(kf), "i"(128 + 4 * n), "i"(131 + 4 * n)); break;
          QS(0) QS(1) QS(2) QS(3) QS(4) QS(5) QS(6) QS(7) QS(8) QS(9) QS(10) QS(11) QS(12) QS(13) QS(14) QS(15)
#undef QS
        }
      }
      asm volatile("s_nop 15\n\ts_nop 15" : "+v"(acc));
      c.sA[u] = acc;
    }
    {
      float pm = c.sA[0][0];
#pragma unroll
      for (int r = 1; r < 16; ++r) pm = fmaxf(pm, c.sA[0][r]);
#pragma unroll
      for (int r = 0; r < 16; ++r) pm = fmaxf(pm, c.sA[1][r]);
      c.m_run = fmaxf(c.m_run, xhalf_max(pm) * SM_C);
    }
    set_mask(t0 + 1);
#pragma unroll
    for (int s = 0; s < 8; ++s) c.kra[s] ^= KBUF_B;
    c.vw ^= KBUF_B;
    SB0();
    __builtin_amdgcn_s_barrier();
    for (int t = t0; t < t1; ++t) {
      const unsigned ks = tclamp(t + 3), vs = tclamp(t + 2);
      c.ps = 0.f;
      frag_reads(c, iseq<LEAD>{});
      SB0();
      gaps(c, rK, rV, ks, vs, iseq<64>{});
      l_run += c.ps;
      const float pmax = xhalf_max(fmaxf(c.mx0, c.mx1)) * SM_C;
      if (!__all(pmax - c.m_run <= RESC_THR)) {
        const float mn = fmaxf(c.m_run, pmax);
        const float alpha = __builtin_amdgcn_exp2f(c.m_run - mn);
        c.m_run = mn;
        l_run *= alpha;
        asm volatile("s_nop 15\n\ts_nop 15" ::: "memory");
        acc_scale(alpha, iseq<128>{});
        asm volatile("s_nop 3" ::: "memory");
      }
      if (t + 2 >= 2 * qb || t + 2 > last) set_mask(t + 2);
      c.sA[0] = c.sB[0]; c.sA[1] = c.sB[1];
#pragma unroll
      for (int s = 0; s < 8; ++s) c.kra[s] ^= KBUF_B;
#pragma unroll
      for (int k4 = 0; k4 < 4; ++k4) c.vra[k4] ^= KBUF_B;
      c.kw0 ^= KBUF_B; c.kw1 ^= KBUF_B; c.vw ^= KBUF_B;
      SB0();
      asm volatile("s_waitcnt lgkmcnt(0)" ::: "memory");
      __builtin_amdgcn_s_barrier();
    }
    asm volatile("s_waitcnt vmcnt(0)\n\ts_nop 15\n\ts_nop 15" ::: "memory");
    {
      auto rr = __builtin_amdgcn_permlane32_swap(__float_as_uint(l_run), __float_as_uint(l_run), false, false);
      l_run = __uint_as_float(rr[0]) + __uint_as_float(rr[1]);
    }
    LDS_AS char* ot = lds + wid * (32 * 528);
    bool do_out = true;
    float a1, a2 = 0.f;
    u32x4 sl[16];
    rsrc_t rot = rK;
    if (partial) {
      const rsrc_t rme = mk_rsrc(slabs + ((size_t)(h * 8 + x) * 2 + role) * SLAB_F4 + wid * (SLAB_F4 / 4), (unsigned)(SLAB_F4 / 4) * 16);
      slab_store(rme, lane * 16, iseq<32>{});
      { const f32x4 ml = {c.m_run, l_run, 0.f, 0.f}; __builtin_amdgcn_raw_buffer_store_b128(*reinterpret_cast<const u32x4*>(&ml), rme, lane * 16, 32 * 1024, 16); }
      asm volatile("s_waitcnt vmcnt(0)" ::: "memory");
      __syncthreads();
      LDS_AS unsigned* flag = (LDS_AS unsigned*)(lds + 4 * 32 * 528);
      if (tid == 0) *flag = __hip_atomic_fetch_add(cnt + h * 8 + x, 1u, __ATOMIC_RELAXED, __HIP_MEMORY_SCOPE_AGENT);
      __syncthreads();
      do_out = (*flag == 1u);
      if (do_out) {
        rot = mk_rsrc(slabs + ((size_t)(h * 8 + x) * 2 + (role ^ 1)) * SLAB_F4 + wid * (SLAB_F4 / 4), (unsigned)(SLAB_F4 / 4) * 16);
        const u32x4 mlu = __builtin_amdgcn_raw_buffer_load_b128(rot, lane * 16, 32 * 1024, 16);
#pragma unroll
        for (int g = 0; g < 16; ++g) sl[g] = __builtin_amdgcn_raw_buffer_load_b128(rot, lane * 16, g * 1024, 16);
        const float m2 = __uint_as_float(mlu[0]), l2 = __uint_as_float(mlu[1]);
        const float mn = fmaxf(c.m_run, m2);
        a1 = __builtin_amdgcn_exp2f(c.m_run - mn); a2 = __builtin_amdgcn_exp2f(m2 - mn);
        const float inv = 1.0f / (l_run * a1 + l2 * a2);
        a1 *= inv; a2 *= inv;
      }
    } else {
      a1 = 1.0f / l_run;
    }
    if (do_out) {
      if (partial) {
        epi_all<true, 0>(ot, sl, lane, a1, a2, iseq<16>{});
        SB0();
#pragma unroll
        for (int g = 0; g < 16; ++g) sl[g] = __builtin_amdgcn_raw_buffer_load_b128(rot, lane * 16, (16 + g) * 1024, 16);
        epi_all<true, 16>(ot, sl, lane, a1, a2, iseq<16>{});
      } else {
        epi_all<false, 0>(ot, sl, lane, a1, a2, iseq<16>{});
        epi_all<false, 16>(ot, sl, lane, a1, a2, iseq<16>{});
      }
      u16* op = ctx + (((size_t)(q0 >> 6) * 64 + h * 4 + ((lane & 31) >> 3)) * 64 + (q0 & 63) + (lane >> 5)) * 64 + (lane & 7) * 8;
#pragma unroll
      for (int it = 0; it < 16; ++it) {
        u32x4 v = *(LDS_AS u32x4*)(ot + (it * 2 + (lane >> 5)) * 528 + (lane & 31) * 16);
        *reinterpret_cast<u32x4*>(op + it * 2 * 64) = v;
      }
    }
  }
}

__device__ __forceinline__ void bdma16(rsrc_t r, unsigned voff, unsigned soff, unsigned lds_dst) {
  asm volatile("s_mov_b32 m0, %0\n\ts_nop 0\n\tbuffer_load_dwordx4 %1, %2, %3 offen lds" :: "s"(lds_dst), "v"(voff), "s"(r), "s"(soff) : "m0", "memory");
}

constexpr int FSTG = 32768, FIN_SHM = 4 * FSTG;
constexpr int FK1 = HDALL / 64, FK2 = FF / 64, FKT = FK1 + FK2;

__global__ __launch_bounds__(512) void final_kernel(const u16* __restrict__ ctx, const u16* __restrict__ wo, const u16* __restrict__ act,
                                                    const u16* __restrict__ w2, const u16* __restrict__ x, u16* __restrict__ out) {
  extern __shared__ __attribute__((aligned(16))) char shm[];
  const int b = blockIdx.x, xi = b >> 3;
  const int pm = (b & 7) * 4 + xi / 7, pn = xi % 7;
  const int brow = pm * 64, bcol = pn * 64, tid = threadIdx.x & 255;
  const int grp = __builtin_amdgcn_readfirstlane(threadIdx.x >> 8);
  const int wid = tid >> 6, lane = tid & 63, wr = wid >> 1, wc = wid & 1, fr = lane & 15, fq = lane >> 4;
  LDS_AS char* lds = (LDS_AS char*)shm + grp * 16384;
  const rsrc_t rC = mk_rsrc(ctx + (size_t)brow * HDALL, 64 * HDALL * 2), rA = mk_rsrc(act + (size_t)brow * FF, 64 * FF * 2);
  const rsrc_t rWo = mk_rsrc(wo, HDALL * MD * 2), rW2 = mk_rsrc(w2, FF * MD * 2);
  const int sr0 = tid >> 3, spc = tid & 7;
  const int acs = spc ^ ((sr0 >> 1) & 7), bcs = spc ^ ((((sr0 >> 1) & 1) | (((sr0 >> 3) & 1) << 1)) << 1);
  const unsigned ac_off = sr0 * 128 + acs * 16, aa_off = sr0 * (FF * 2) + acs * 16;
  const unsigned b_off = sr0 * (MD * 2) + bcol * 2 + bcs * 16;
  const unsigned ldsw = (unsigned)(uintptr_t)lds + __builtin_amdgcn_readfirstlane(wid) * 1024;
  auto dma = [&](int j) {
    const int kt = 2 * j + grp;
    const int kc = kt < FKT ? kt : FKT - 1;
    const unsigned dst = ldsw + (j & 3) * FSTG;
    if (kc < FK1) {
      bdma16(rC, ac_off, kc * 8192, dst); bdma16(rC, ac_off, kc * 8192 + 4096, dst + 4096);
      bdma16(rWo, b_off, kc * 64 * MD * 2, dst + 8192); bdma16(rWo, b_off, (kc * 64 + 32) * MD * 2, dst + 12288);
    } else {
      const int k2 = kc - FK1;
      bdma16(rA, aa_off, k2 * 128, dst); bdma16(rA, aa_off, k2 * 128 + 32 * FF * 2, dst + 4096);
      bdma16(rW2, b_off, k2 * 64 * MD * 2, dst + 8192); bdma16(rW2, b_off, (k2 * 64 + 32) * MD * 2, dst + 12288);
    }
  };
  const int bq = (lane >> 2) & 3, bp = lane & 3;
  int aoff[2], boff[2];
#pragma unroll
  for (int m = 0; m < 2; ++m) { int r = wr * 32 + m * 16 + fr; aoff[m] = r * 128 + ((fq ^ ((r >> 1) & 7)) << 4); }
#pragma unroll
  for (int n = 0; n < 2; ++n) {
    int r = fq * 8 + bq, f = ((r >> 1) & 1) | (((r >> 3) & 1) << 1), cp = wc * 2 + n;
    boff[n] = 8192 + r * 128 + (((cp * 2 + (bp >> 1)) ^ (f << 1)) << 4) + (bp & 1) * 8;
  }
  struct Frag { bf16x8 a[2][2], b[2][2]; };
  auto rdfrag = [&](int slot, Frag& f) {
    LDS_AS char* sb = lds + slot * FSTG;
#pragma unroll
    for (int kk = 0; kk < 2; ++kk) {
#pragma unroll
      for (int m = 0; m < 2; ++m) f.a[kk][m] = *(LDS_AS bf16x8*)(sb + (aoff[m] ^ (kk * 64)));
#pragma unroll
      for (int n = 0; n < 2; ++n) {
        LDS_AS char* p = sb + boff[n] + kk * 4096;
        s16x4 lo = __builtin_amdgcn_ds_read_tr16_b64_v4i16((LDS_AS s16x4*)p);
        s16x4 hh = __builtin_amdgcn_ds_read_tr16_b64_v4i16((LDS_AS s16x4*)(p + 512));
        f.b[kk][n] = (bf16x8){lo[0], lo[1], lo[2], lo[3], hh[0], hh[1], hh[2], hh[3]};
      }
    }
  };
  f32x4 acc[2][2] = {}, accs[2][2];
  auto mma = [&](const Frag& f) {
#pragma unroll
    for (int kk = 0; kk < 2; ++kk)
#pragma unroll
      for (int m = 0; m < 2; ++m)
#pragma unroll
        for (int n = 0; n < 2; ++n) acc[m][n] = __builtin_amdgcn_mfma_f32_16x16x32_bf16(f.b[kk][n], f.a[kk][m], acc[m][n], 0, 0, 0);
  };
  Frag F0, F1;
  dma(0); dma(1); dma(2); dma(3);
  asm volatile("s_waitcnt vmcnt(12)" ::: "memory");
  __builtin_amdgcn_s_barrier();
  rdfrag(0, F0);
#define FSTEP(j, I, FC, FN) do {                                                                                       \
    asm volatile("s_waitcnt vmcnt(8) lgkmcnt(0)" ::: "memory"); __builtin_amdgcn_s_barrier();                         \
    rdfrag(((I) + 1) & 3, FN); dma((j) + 4);                                                                           \
    __builtin_amdgcn_sched_barrier(0); if ((j) < FKT / 2) mma(FC); __builtin_amdgcn_sched_barrier(0); } while (0)
  auto four = [&](int j) { FSTEP(j, 0, F0, F1); FSTEP(j + 1, 1, F1, F0); FSTEP(j + 2, 2, F0, F1); FSTEP(j + 3, 3, F1, F0); };
  for (int kt = 0; kt < FK1 / 2; kt += 4) four(kt);
#pragma unroll
  for (int m = 0; m < 2; ++m)
#pragma unroll
    for (int n = 0; n < 2; ++n) { accs[m][n] = acc[m][n]; acc[m][n] = (f32x4){0.f, 0.f, 0.f, 0.f}; }
  for (int kt = FK1 / 2; kt < FKT / 2; kt += 4) four(kt);
#undef FSTEP
  asm volatile("s_waitcnt vmcnt(0) lgkmcnt(0)" ::: "memory");
  __builtin_amdgcn_s_barrier();
  {
    LDS_AS f32x4* red = (LDS_AS f32x4*)shm;
    if (grp == 1) {
#pragma unroll
      for (int m = 0; m < 2; ++m)
#pragma unroll
        for (int n = 0; n < 2; ++n) { red[(m * 2 + n) * 256 + tid] = accs[m][n]; red[(4 + m * 2 + n) * 256 + tid] = acc[m][n]; }
    }
    __syncthreads();
    if (grp == 1) return;
#pragma unroll
    for (int m = 0; m < 2; ++m)
#pragma unroll
      for (int n = 0; n < 2; ++n) { accs[m][n] += red[(m * 2 + n) * 256 + tid]; acc[m][n] += red[(4 + m * 2 + n) * 256 + tid]; }
  }
#pragma unroll
  for (int m = 0; m < 2; ++m)
#pragma unroll
    for (int n = 0; n < 2; ++n) {
      int row = brow + wr * 32 + m * 16 + fr, col = bcol + wc * 32 + n * 16 + fq * 4;
      s16x4 xv = *reinterpret_cast<const s16x4*>(x + (size_t)row * MD + col);
      float r[4];
#pragma unroll
      for (int j = 0; j < 4; ++j) {
        float s = bfr(accs[m][n][j]);
        float y = bfr(acc[m][n][j]);
        float g = bfr(y / (1.0f + expf(-y)));
        r[j] = bf2f((u16)xv[j]) + bfr(s * g);
      }
      u32x2 w = {pk2(r[0], r[1]), pk2(r[2], r[3])};
      *reinterpret_cast<u32x2*>(out + (size_t)row * MD + col) = w;
    }
}

extern "C" void kernel_launch(void* const* d_in, const int* in_sizes, int n_in, void* d_out, int out_size, void* d_ws, size_t ws_size,
                              hipStream_t stream) {
  const u16* x = (const u16*)d_in[0];
  const u16* ckv = (const u16*)d_in[1];
  const u16* wq = (const u16*)d_in[2];
  const u16* wuk = (const u16*)d_in[3];
  const u16* wuv = (const u16*)d_in[4];
  const u16* wo = (const u16*)d_in[5];
  const u16* w1 = (const u16*)d_in[7];
  const u16* b1 = (const u16*)d_in[8];
  const u16* w2 = (const u16*)d_in[9];
  char* ws = (char*)d_ws;
  u16* xn = (u16*)(ws + WS_XN); u16* Q = (u16*)(ws + WS_Q); u16* K = (u16*)(ws + WS_K); u16* Vt = (u16*)(ws + WS_VT);
  u16* act = (u16*)(ws + WS_ACT); u16* ctx = (u16*)(ws + WS_CTX);
  hipFuncSetAttribute((const void*)proj_gemm_kernel, hipFuncAttributeMaxDynamicSharedMemorySize, GEMM_SHM);
  hipFuncSetAttribute((const void*)attn_kernel, hipFuncAttributeMaxDynamicSharedMemorySize, ATT_SHM);
  ln_kernel<<<SQ / 4, 256, 0, stream>>>(x, xn, (u16*)(ws + WS_LUT));
  proj_gemm_kernel<<<N_GEMM_TILES, 512, GEMM_SHM, stream>>>(ckv, xn, wq, wuk, wuv, w1, b1, Q, K, Vt, act, (unsigned*)(ws + WS_CNT));
  attn_kernel<<<256, 256, ATT_SHM, stream>>>(Q, K, Vt, ctx, act, (f32x4*)(ws + WS_SLAB), (unsigned*)(ws + WS_CNT), (const u16*)(ws + WS_LUT));
  hipFuncSetAttribute((const void*)final_kernel, hipFuncAttributeMaxDynamicSharedMemorySize, FIN_SHM);
  final_kernel<<<(SQ / 64) * 7, 512, FIN_SHM, stream>>>(ctx, wo, act, w2, x, (u16*)d_out);
}
```

```cpp
#include <hip/hip_runtime.h>
#include <hip/hip_bf16.h>
#include <stdint.h>
#include <utility>

typedef short bf16x8 __attribute__((ext_vector_type(8)));
typedef short s16x4 __attribute__((ext_vector_type(4)));
typedef float f32x4 __attribute__((ext_vector_type(4)));
typedef float f32x16 __attribute__((ext_vector_type(16)));
typedef unsigned u32x2 __attribute__((ext_vector_type(2)));
typedef unsigned u32x4 __attribute__((ext_vector_type(4)));
typedef unsigned short u16;
#define LDS_AS __attribute__((address_space(3)))

constexpr int SQ = 2048, MD = 448, MDP = 512, DC = 1024, NH = 16, HD = 256, HDALL = NH * HD, FF = 1792;

constexpr size_t WS_XN = 0;
constexpr size_t WS_Q = WS_XN + (size_t)SQ * MDP * 2;
constexpr size_t WS_K = WS_Q + (size_t)NH * SQ * HD * 2;
constexpr size_t WS_VT = WS_K + (size_t)NH * SQ * HD * 2;
constexpr size_t WS_ACT = WS_VT + (size_t)NH * SQ * HD * 2;
constexpr size_t WS_CTX = WS_ACT + (size_t)SQ * FF * 2;
constexpr size_t WS_SLAB = WS_CTX + (size_t)SQ * HDALL * 2;
constexpr size_t WS_CNT = WS_SLAB + (size_t)256 * 4 * (32 * 64 + 64) * 16;
constexpr size_t WS_LUT = WS_CNT + 1024;
constexpr size_t WS_END = WS_LUT + 65536 * 2;

typedef __amdgpu_buffer_rsrc_t rsrc_t;
__device__ __forceinline__ rsrc_t mk_rsrc(const void* p, unsigned bytes) {
  return __builtin_amdgcn_make_buffer_rsrc((void*)p, 0, bytes, 0x00020000);
}
__device__ __forceinline__ float bf2f(u16 v) { return __uint_as_float(((unsigned)v) << 16); }
__device__ __forceinline__ u16 f2bf(float f) { __hip_bfloat16 b = __float2bfloat16(f); return *reinterpret_cast<u16*>(&b); }
__device__ __forceinline__ float bfr(float f) { return bf2f(f2bf(f)); }
__device__ __forceinline__ unsigned pk2(float lo, float hi) {
  unsigned r; asm("v_cvt_pk_bf16_f32 %0, %1, %2" : "=v"(r) : "v"(lo), "v"(hi)); return r;
}


__device__ __forceinline__ float mish_ref(float h) {
  const float L2E = 1.4426950408889634f, LN2 = 0.6931471805599453f;
  const float e = bfr(__builtin_amdgcn_exp2f(-fabsf(h) * L2E));
  const float lbig = __builtin_amdgcn_logf(1.0f + e) * LN2;
  const float lsml = e * fmaf(-e, fmaf(-e, 0.33333334f, 0.5f), 1.0f);
  const float l = bfr(e < 0.015625f ? lsml : lbig);
  const float sp = bfr(l + fmaxf(h, 0.f));
  const float w = __builtin_amdgcn_exp2f(sp * (-2.0f * L2E));
  const float tbig = (1.0f - w) * __builtin_amdgcn_rcpf(1.0f + w);
  const float tsml = sp * fmaf(sp * sp, -0.33333334f, 1.0f);
  const float th = bfr(sp < 0.03125f ? tsml : tbig);
  return h * th;
}

__global__ __launch_bounds__(256) void ln_kernel(const u16* __restrict__ x, u16* __restrict__ xn, u16* __restrict__ lut, unsigned* __restrict__ cnt) {
  if (blockIdx.x == 0 && threadIdx.x < 129) cnt[threadIdx.x] = 0u;
  const int row = blockIdx.x * 4 + (threadIdx.x >> 6), lane = threadIdx.x & 63;
  { const int gid = blockIdx.x * 256 + threadIdx.x; if (gid < 65536) lut[gid] = f2bf(mish_ref(bf2f((u16)gid))); }
  float v[8];
  const bool act = lane < 56;
  if (act) {
    bf16x8 r = *reinterpret_cast<const bf16x8*>(x + (size_t)row * MD + lane * 8);
#pragma unroll
    for (int j = 0; j < 8; ++j) v[j] = bf2f((u16)r[j]);
  } else {
#pragma unroll
    for (int j = 0; j < 8; ++j) v[j] = 0.f;
  }
  float s = 0.f;
#pragma unroll
  for (int j = 0; j < 8; ++j) s += v[j];
#pragma unroll
  for (int o = 32; o >= 1; o >>= 1) s += __shfl_xor(s, o);
  const float mean = s * (1.0f / MD);
  float q = 0.f;
  if (act) {
#pragma unroll
    for (int j = 0; j < 8; ++j) { float d = v[j] - mean; q += d * d; }
  }
#pragma unroll
  for (int o = 32; o >= 1; o >>= 1) q += __shfl_xor(q, o);
  const float mb = bfr(mean), vb = bfr(q * (1.0f / MD));
  const float den = bfr(sqrtf(bfr(vb + 1e-5f)));
  bf16x8 o8;
#pragma unroll
  for (int j = 0; j < 8; ++j) o8[j] = act ? (short)f2bf(bfr(v[j] - mb) / den) : (short)0;
  *reinterpret_cast<bf16x8*>(xn + (size_t)row * MDP + lane * 8) = o8;
}

constexpr int BM = 256, BK = 64, HALF = 128, HT = HALF * BK, GEMM_SHM = 8 * HT * 2;

__device__ __forceinline__ int lds_byte(int r, int c) {
  int st = (r >> 4) * 2 + (c >> 5), rr = r & 15, cc = c & 31, ob = rr * 64 + cc * 2;
  return st * 1024 + (ob ^ (((ob >> 9) & 1) << 5));
}
__device__ __forceinline__ void stage_rc(int b, int& R, int& C) {
  int st = b / 1024, sb = b % 1024, swz = sb ^ (((sb >> 9) & 1) << 5);
  R = (st >> 1) * 16 + swz / 64; C = (st & 1) * 32 + (swz % 64) / 2;
}

enum { EPI_ROWMAJOR = 0, EPI_VT = 1, EPI_MISH = 2 };

template <int EPI>
__device__ __forceinline__ void gemm_tile(const u16* __restrict__ A, int lda, const u16* __restrict__ Bg, int ldb, int kmax, int nt,
                                          int brow, int bcol, u16* __restrict__ out, int ldo, const u16* __restrict__ bias, char* shm_raw) {
  u16* shm = (u16*)shm_raw;
#define SA(b, h) (shm + ((b) * 2 + (h)) * HT)
#define SB(b, h) (shm + (4 + (b) * 2 + (h)) * HT)
#define STAGE_A(T, P, br, kt) do { const u16* _g = A + (long)(br) * lda + (long)(kt) * BK;                     \
    for (int _i = 0; _i < 2; ++_i) { int _b = (T) * 16 + _i * 8192; int _r, _c; stage_rc(_b, _r, _c);          \
      __builtin_amdgcn_global_load_lds((const unsigned*)(_g + (long)_r * lda + _c), (LDS_AS unsigned*)((char*)(P) + _b), 16, 0, 0); } } while (0)
#define STAGE_B(T, P, bc, kt) do {                                                                              \
    for (int _i = 0; _i < 2; ++_i) { int _b = (T) * 16 + _i * 8192; int _k = _b >> 8, _pc = (_b >> 4) & 15;     \
      int _c = _pc ^ (((_k & 3) | (((_k >> 3) & 1) << 2)) << 1); int _kr = (kt) * BK + _k; _kr = _kr < kmax ? _kr : kmax - 1; \
      __builtin_amdgcn_global_load_lds((const unsigned*)(Bg + (long)_kr * ldb + (bc) + _c * 8), (LDS_AS unsigned*)((char*)(P) + _b), 16, 0, 0); } } while (0)
#define LDA(dst, b, h) for (int m = 0; m < 4; ++m) for (int k = 0; k < 2; ++k) \
    dst[m][k] = *reinterpret_cast<const bf16x8*>((char*)SA(b, h) + lds_byte(wr * 64 + m * 16 + fr, k * 32 + fq * 8))
#define LDB(dst, b, h) for (int n = 0; n < 2; ++n) for (int k = 0; k < 2; ++k) {                                \
    LDS_AS char* _p = (LDS_AS char*)SB(b, h) + bbase + n * 32 + k * (32 * 256);                                 \
    _p = (LDS_AS char*)((unsigned)(uintptr_t)(LDS_AS char*)SB(b, h) + ((bbase + n * 32) ^ bxor) + k * (32 * 256)); \
    s16x4 _lo = __builtin_amdgcn_ds_read_tr16_b64_v4i16((LDS_AS s16x4*)_p);                                     \
    s16x4 _hi = __builtin_amdgcn_ds_read_tr16_b64_v4i16((LDS_AS s16x4*)(_p + 1024));                            \
    dst[n][k] = (bf16x8){_lo[0], _lo[1], _lo[2], _lo[3], _hi[0], _hi[1], _hi[2], _hi[3]}; }
#define MMA(ai, bj, At, Bt) do { __builtin_amdgcn_s_setprio(1);                                                 \
    for (int m = 0; m < 4; ++m) for (int n = 0; n < 2; ++n) for (int k = 0; k < 2; ++k) {                         \
      if constexpr (EPI == EPI_VT) acc[ai][bj][m][n] = __builtin_amdgcn_mfma_f32_16x16x32_bf16(At[m][k], Bt[n][k], acc[ai][bj][m][n], 0, 0, 0); \
      else acc[ai][bj][m][n] = __builtin_amdgcn_mfma_f32_16x16x32_bf16(Bt[n][k], At[m][k], acc[ai][bj][m][n], 0, 0, 0); }                     \
    __builtin_amdgcn_s_setprio(0); } while (0)
#define WAIT_V(n) asm volatile("s_waitcnt vmcnt(" #n ")" ::: "memory")
#define WAIT_L(n) asm volatile("s_waitcnt lgkmcnt(" #n ")" ::: "memory")
#define BAR __builtin_amdgcn_s_barrier()
#define SCHED __builtin_amdgcn_sched_barrier(0)
  int tid = threadIdx.x;
  int wid = tid >> 6, lane = tid & 63, wr = wid >> 2, wc = wid & 3, fr = lane & 15, fq = lane >> 4;
  int bq = (lane >> 2) & 3, bp = lane & 3;
  int bbase = (fq * 8 + bq) * 256 + (wc * 4 + (bp >> 1)) * 16 + (bp & 1) * 8;
  int bxor = (bq | ((fq & 1) << 2)) << 5;
  f32x4 acc[2][2][4][2] = {};
  bf16x8 At[4][2], B0[2][2], B1[2][2];
  STAGE_B(tid, SB(0, 0), bcol, 0); STAGE_A(tid, SA(0, 0), brow, 0);
  STAGE_B(tid, SB(0, 1), bcol + HALF, 0); STAGE_A(tid, SA(0, 1), brow + HALF, 0);
  if (wr == 1) BAR;
  WAIT_V(4); BAR;
  STAGE_B(tid, SB(1, 0), bcol, 1); STAGE_A(tid, SA(1, 0), brow, 1); STAGE_B(tid, SB(1, 1), bcol + HALF, 1);
  WAIT_V(6); BAR;
  for (int t = 0; t < nt - 2; t += 2) {
    LDB(B0, 0, 0); SCHED; LDA(At, 0, 0); STAGE_A(tid, SA(1, 1), brow + HALF, t + 1);
    WAIT_L(8); BAR; WAIT_L(0); SCHED; MMA(0, 0, At, B0); BAR; SCHED;
    LDB(B1, 0, 1); STAGE_B(tid, SB(0, 0), bcol, t + 2);
    BAR; WAIT_L(0); SCHED; MMA(0, 1, At, B1); BAR;
    LDA(At, 0, 1); STAGE_A(tid, SA(0, 0), brow, t + 2);
    BAR; WAIT_L(0); SCHED; MMA(1, 0, At, B0); BAR; SCHED;
    STAGE_B(tid, SB(0, 1), bcol + HALF, t + 2);
    WAIT_V(6); BAR; MMA(1, 1, At, B1); BAR;
    LDB(B0, 1, 0); SCHED; LDA(At, 1, 0); STAGE_A(tid, SA(0, 1), brow + HALF, t + 2);
    WAIT_L(8); BAR; WAIT_L(0); SCHED; MMA(0, 0, At, B0); BAR; SCHED;
    LDB(B1, 1, 1); STAGE_B(tid, SB(1, 0), bcol, t + 3);
    BAR; WAIT_L(0); SCHED; MMA(0, 1, At, B1); BAR;
    LDA(At, 1, 1); STAGE_A(tid, SA(1, 0), brow, t + 3);
    BAR; WAIT_L(0); SCHED; MMA(1, 0, At, B0); BAR; SCHED;
    STAGE_B(tid, SB(1, 1), bcol + HALF, t + 3);
    WAIT_V(6); BAR; MMA(1, 1, At, B1); BAR;
  }
  int tz = threadIdx.x; asm volatile("" : "+v"(tz));
  wid = tz >> 6; lane = tz & 63; wr = wid >> 2; wc = wid & 3; fr = lane & 15; fq = lane >> 4;
  bq = (lane >> 2) & 3; bp = lane & 3;
  bbase = (fq * 8 + bq) * 256 + (wc * 4 + (bp >> 1)) * 16 + (bp & 1) * 8;
  bxor = (bq | ((fq & 1) << 2)) << 5;
  { LDB(B0, 0, 0); WAIT_V(0); LDA(At, 0, 0); STAGE_A(tz, SA(1, 1), brow + HALF, nt - 1);
    BAR; WAIT_L(0); SCHED; MMA(0, 0, At, B0); BAR;
    LDB(B1, 0, 1); BAR; WAIT_L(0); SCHED; MMA(0, 1, At, B1); BAR;
    LDA(At, 0, 1); WAIT_V(4); BAR; WAIT_L(0); SCHED; MMA(1, 0, At, B0); MMA(1, 1, At, B1); BAR; }
  const bool real_last = nt * BK <= kmax;
  { LDB(B0, 1, 0); LDA(At, 1, 0); WAIT_V(2); BAR; WAIT_L(0); SCHED; if (real_last) MMA(0, 0, At, B0); BAR;
    LDB(B1, 1, 1); WAIT_V(0); BAR; WAIT_L(0); SCHED; if (real_last) MMA(0, 1, At, B1); BAR;
    LDA(At, 1, 1); BAR; WAIT_L(0); SCHED; if (real_last) { MMA(1, 0, At, B0); MMA(1, 1, At, B1); } BAR; }
  if (wr == 0) BAR;
  __syncthreads();
  LDS_AS char* ep = (LDS_AS char*)shm_raw;
#pragma unroll
  for (int ai = 0; ai < 2; ++ai)
#pragma unroll
    for (int bj = 0; bj < 2; ++bj)
#pragma unroll
      for (int m = 0; m < 4; ++m)
#pragma unroll
        for (int n = 0; n < 2; ++n) {
          f32x4 a = acc[ai][bj][m][n];
          int row, col;
          if constexpr (EPI == EPI_VT) {
            row = bj * HALF + wc * 32 + n * 16 + fr;
            col = ai * HALF + wr * 64 + m * 16 + (((fq & 1) << 1) | (fq >> 1)) * 4;
          } else {
            row = ai * HALF + wr * 64 + m * 16 + fr;
            col = bj * HALF + wc * 32 + n * 16 + fq * 4;
            if constexpr (EPI == EPI_MISH) {
              s16x4 bb = *reinterpret_cast<const s16x4*>(bias + bcol + col);
#pragma unroll
              for (int j = 0; j < 4; ++j) a[j] = bfr(a[j]) + bf2f((u16)bb[j]);
            }
          }
          u32x2 w = {pk2(a[0], a[1]), pk2(a[2], a[3])};
          *(LDS_AS u32x2*)(ep + row * 512 + ((col * 2) ^ ((row & 31) << 4))) = w;
        }
  __syncthreads();
  {
    const int l31 = lane & 31, rsub = lane >> 5;
#pragma unroll
    for (int it = 0; it < 16; ++it) {
      const int row = wid * 32 + it * 2 + rsub;
      const u32x4 v = *(LDS_AS u32x4*)(ep + row * 512 + ((l31 ^ (row & 31)) << 4));
      if constexpr (EPI == EPI_VT) {
        *reinterpret_cast<u32x4*>(out + (size_t)((brow >> 6) + (l31 >> 3)) * (HD * 64) + row * 64 + (l31 & 7) * 8) = v;
      } else if constexpr (EPI == EPI_MISH) {
        __builtin_amdgcn_raw_buffer_store_b128(v, mk_rsrc(out, SQ * FF * 2), (unsigned)((brow + row) * ldo + bcol + l31 * 8) * 2, 0, 16);
      } else {
        *reinterpret_cast<u32x4*>(out + (size_t)(brow + row) * ldo + l31 * 8) = v;
      }
    }
  }
#undef SA
#undef SB
#undef STAGE_A
#undef STAGE_B
#undef LDA
#undef LDB
#undef MMA
}

constexpr int N_KV_TILES = 256, N_Q_TILES = 128, N_UP_TILES = 56, N_MISH = 64;
constexpr int N_GEMM_BLOCKS = N_UP_TILES + N_KV_TILES + N_Q_TILES + N_MISH;

__global__ __launch_bounds__(512, 2) void proj_gemm_kernel(const u16* __restrict__ ckv, const u16* __restrict__ xn, const u16* __restrict__ wq,
                                                           const u16* __restrict__ wuk, const u16* __restrict__ wuv, const u16* __restrict__ w1,
                                                           const u16* __restrict__ b1, u16* __restrict__ Qo, u16* __restrict__ Ko,
                                                           u16* __restrict__ Vto, u16* __restrict__ acto, unsigned* __restrict__ cnt,
                                                           const u16* __restrict__ lut) {
  extern __shared__ __attribute__((aligned(16))) char shm[];
  const int b = blockIdx.x;
  if (b < N_UP_TILES) {
    const int pm = b & 7, pn = b >> 3;
    gemm_tile<EPI_MISH>(xn, MDP, w1, FF, MD, MDP / BK, pm * BM, pn * BM, acto, FF, b1, shm);
    asm volatile("s_waitcnt vmcnt(0)" ::: "memory");
    __syncthreads();
    if (threadIdx.x == 0) __hip_atomic_fetch_add(cnt + 128, 1u, __ATOMIC_RELAXED, __HIP_MEMORY_SCOPE_AGENT);
  } else if (b < N_UP_TILES + N_KV_TILES + N_Q_TILES) {
    const bool isq = b >= N_UP_TILES + N_KV_TILES;
    const int j = b - N_UP_TILES - (isq ? N_KV_TILES : 0), xcd = j & 7, i = j >> 3;
    const int pn = isq ? xcd * 2 + (i & 1) : xcd * 4 + (i & 3), pm = isq ? i >> 1 : i >> 2;
    if (!isq && pn >= 16) {
      gemm_tile<EPI_VT>(ckv, DC, wuv, HDALL, DC, DC / BK, pm * BM, (pn - 16) * BM, Vto + (size_t)(pn - 16) * SQ * HD, SQ, nullptr, shm);
    } else {
      gemm_tile<EPI_ROWMAJOR>(isq ? xn : ckv, isq ? MDP : DC, isq ? wq : wuk, HDALL, isq ? MD : DC, isq ? MDP / BK : DC / BK, pm * BM, pn * BM,
                              (isq ? Qo : Ko) + (size_t)pn * SQ * HD, HD, nullptr, shm);
    }
  } else {
    const int j = b - (N_UP_TILES + N_KV_TILES + N_Q_TILES), tid = threadIdx.x;
    LDS_AS u16* tb = (LDS_AS u16*)shm;
#pragma unroll
    for (int i = 0; i < 2; ++i) {
      const int e8 = i * 512 + tid;
      const int code = ((e8 >> 9) << 15) | (((103 << 7) + (e8 & 511) * 8));
      *(LDS_AS bf16x8*)(tb + e8 * 8) = *reinterpret_cast<const bf16x8*>(lut + code);
    }
    if (tid == 0) {
      for (unsigned spin = 0; spin < (1u << 24) && __hip_atomic_load(cnt + 128, __ATOMIC_RELAXED, __HIP_MEMORY_SCOPE_AGENT) < (unsigned)N_UP_TILES; ++spin)
        __builtin_amdgcn_s_sleep(8);
    }
    __syncthreads();
    constexpr int CH = SQ * FF / 8 / N_MISH;
    const rsrc_t rh = mk_rsrc(acto + (size_t)j * CH * 8, CH * 16);
#pragma unroll 1
    for (int i0 = 0; i0 < CH / 512; i0 += 7) {
      u32x4 hv[7];
#pragma unroll
      for (int i = 0; i < 7; ++i) hv[i] = __builtin_amdgcn_raw_buffer_load_b128(rh, tid * 16, (i0 + i) * 8192, 16);
#pragma unroll
      for (int i = 0; i < 7; ++i) {
        const bf16x8 h8 = *reinterpret_cast<const bf16x8*>(&hv[i]);
        bf16x8 w;
        bool oob = false;
#pragma unroll
        for (int e = 0; e < 8; ++e) {
          const unsigned hc = (u16)h8[e];
          const unsigned idx = (hc & 0x7fff) - (103u << 7);
          oob |= idx >= 4096u;
          w[e] = (short)tb[((hc >> 15) << 12) + (idx & 4095u)];
        }
        if (oob) {
#pragma unroll
          for (int e = 0; e < 8; ++e) w[e] = (short)f2bf(mish_ref(bf2f((u16)h8[e])));
        }
        *reinterpret_cast<bf16x8*>(acto + ((size_t)j * CH + (i0 + i) * 512 + tid) * 8) = w;
      }
    }
  }
}

constexpr int KT = 64;
constexpr int KBUF_B = KT * HD * 2;
constexpr int ATT_SHM = 4 * KBUF_B;
constexpr float SM_C = 0.0625f * 1.4426950408889634f;
constexpr float RESC_THR = 8.0f;
constexpr int SPLIT_T_ = 18;

template <int N> using iseq = std::make_integer_sequence<int, N>;
#define SB0() __builtin_amdgcn_sched_barrier(0)

template <int I> __device__ __forceinline__ void acc_zero1() { asm volatile("v_accvgpr_write_b32 a[%c0], 0" :: "i"(I)); }
template <int... Is> __device__ __forceinline__ void acc_zero(std::integer_sequence<int, Is...>) { (acc_zero1<Is>(), ...); }
template <int I> __device__ __forceinline__ void acc_scale1(float alpha) {
  float tmp;
  asm volatile("v_accvgpr_read_b32 %0, a[%c2]\n\tv_mul_f32 %0, %0, %1\n\tv_accvgpr_write_b32 a[%c2], %0" : "=&v"(tmp) : "v"(alpha), "i"(I));
}
template <int... Is> __device__ __forceinline__ void acc_scale(float alpha, std::integer_sequence<int, Is...>) { (acc_scale1<Is>(alpha), ...); }
template <int I> __device__ __forceinline__ float acc_read() { float x; asm volatile("v_accvgpr_read_b32 %0, a[%c1]" : "=v"(x) : "i"(I)); return x; }
template <int R> __device__ __forceinline__ f32x4 acc_read4() { return (f32x4){acc_read<R>(), acc_read<R + 1>(), acc_read<R + 2>(), acc_read<R + 3>()}; }
template <int S> __device__ __forceinline__ void q_load1(rsrc_t r, unsigned voff, unsigned soff) {
  asm volatile("s_nop 4\n\tbuffer_load_dwordx4 a[%c3:%c4], %0, %1, %2 offen" :: "v"(voff), "s"(r), "s"(soff + S * 32), "i"(128 + 4 * S), "i"(131 + 4 * S) : "memory");
}
template <int... Ss> __device__ __forceinline__ void q_load(rsrc_t r, unsigned voff, unsigned soff, std::integer_sequence<int, Ss...>) { (q_load1<Ss>(r, voff, soff), ...); }
template <int J, int SLOT> __device__ __forceinline__ void stage_step(unsigned waddr, rsrc_t r, unsigned voff, unsigned soff) {
  constexpr int OFF = (J < 8 ? (J >> 1) * 8192 : (J - 8) * 4096) + SLOT * KBUF_B;
  asm volatile("s_waitcnt vmcnt(15)\n\tds_write_b128 %0, a[%c1:%c2] offset:%c3\n\tbuffer_load_dwordx4 a[%c1:%c2], %4, %5, %6 offen"
               :: "v"(waddr), "i"(192 + 4 * J), "i"(195 + 4 * J), "i"(OFF), "v"(voff), "s"(r), "s"(soff) : "memory");
}
template <int J> __device__ __forceinline__ void stage_load(rsrc_t r, unsigned voff, unsigned soff) {
  asm volatile("s_nop 4\n\tbuffer_load_dwordx4 a[%c0:%c1], %2, %3, %4 offen" :: "i"(192 + 4 * J), "i"(195 + 4 * J), "v"(voff), "s"(r), "s"(soff) : "memory");
}
template <int J> __device__ __forceinline__ void stage_write(unsigned waddr) {
  constexpr int OFF = J < 8 ? (J >> 1) * 8192 : (J - 8) * 4096;
  asm volatile("ds_write_b128 %0, a[%c1:%c2] offset:%c3" :: "v"(waddr), "i"(192 + 4 * J), "i"(195 + 4 * J), "i"(OFF) : "memory");
}
template <int J0, int... Js> __device__ __forceinline__ void stage_load8(rsrc_t r, unsigned voff, unsigned soff, std::integer_sequence<int, Js...>) {
  (stage_load<J0 + Js>(r, voff, soff + Js * 4096), ...);
}

constexpr int LEAD = 7;
struct AttnRegs {
  f32x16 S[2][2], cin[2];
  bf16x8 fr[8];
  unsigned pw[16];
  float pe[2];
  float ps, m_run, mx0, mx1;
  unsigned kra[8], vra[4];
  unsigned kw0, kw1, vw, kg, vg;
};
template <int P, int F> __device__ __forceinline__ void frag_read(AttnRegs& c) {
  if constexpr (F < 32) {
    asm volatile("ds_read_b128 %0, %1 offset:%c2" : "=v"(c.fr[F & 7]) : "v"(c.kra[F & 7]), "i"((P ^ 1) * KBUF_B + (F >> 4) * 16384 + ((F >> 3) & 1) * 256) : "memory");
  } else {
    asm volatile("ds_read_b128 %0, %1 offset:%c2" : "=v"(c.fr[F & 7]) : "v"(c.vra[(F - 32) >> 3]), "i"(P * KBUF_B + ((F - 32) & 7) * 4096) : "memory");
  }
}
template <int P, int G> __device__ __forceinline__ void gap_mfma(AttnRegs& c) {
  constexpr int W = (62 - G) < (LEAD - 2) ? (62 - G) : (LEAD - 2);
  if constexpr ((G & 1) == 0) asm volatile("s_waitcnt lgkmcnt(%c0)" :: "i"(W) : "memory");
  if constexpr (G < 32) {
    constexpr int U = G >> 4, S = G & 15;
    if constexpr (S == 0)
      asm volatile("v_mfma_f32_32x32x16_bf16 %0, %1, a[128:131], %2" : "=&v"(c.S[P ^ 1][U]) : "v"(c.fr[G & 7]), "v"(c.cin[U]));
    else
      asm volatile("v_mfma_f32_32x32x16_bf16 %0, %1, a[%c2:%c3], %0" : "+v"(c.S[P ^ 1][U]) : "v"(c.fr[G & 7]), "i"(128 + 4 * S), "i"(131 + 4 * S));
  } else {
    constexpr int KS = (G - 32) >> 3, D = (G - 32) & 7;
    u32x4 pv = {c.pw[4 * KS], c.pw[4 * KS + 1], c.pw[4 * KS + 2], c.pw[4 * KS + 3]};
    asm volatile("v_mfma_f32_32x32x16_bf16 a[%c2:%c3], %0, %1, a[%c2:%c3]" :: "v"(c.fr[G & 7]), "v"(pv), "i"(D * 16), "i"(D * 16 + 15));
  }
}
template <int P, int E> __device__ __forceinline__ void sm_elem(AttnRegs& c) {
  const float p = __builtin_amdgcn_exp2f(fmaf(c.S[P][E >> 4][E & 15], SM_C, -c.m_run));
  c.ps += p;
  if constexpr (E & 1) c.pw[E >> 1] = pk2(c.pe[0], p); else c.pe[0] = p;
}
constexpr int elem_gap(int e) { return (e * 17) / 10; }
template <int P, int G, int E> __device__ __forceinline__ void sm_for_gap(AttnRegs& c) {
  if constexpr (E < 32) {
    if constexpr (elem_gap(E) == G) sm_elem<P, E>(c);
    if constexpr (elem_gap(E) <= G) sm_for_gap<P, G, E + 1>(c);
  }
}
template <int P, int G> __device__ __forceinline__ void gap(AttnRegs& c, rsrc_t rK, rsrc_t rV, unsigned ks, unsigned vs) {
  gap_mfma<P, G>(c);
  if constexpr (G + LEAD < 64) frag_read<P, G + LEAD>(c);
  sm_for_gap<P, G, 0>(c);
  if constexpr (G < 32 && (G & 3) == 2) { constexpr int J = G >> 2; stage_step<J, P>((J & 1) ? c.kw1 : c.kw0, rK, c.kg, ks + J * 4096); }
  if constexpr (G >= 32 && (G & 3) == 2) { constexpr int J = (G - 32) >> 2; stage_step<8 + J, P ^ 1>(c.vw, rV, c.vg, vs + J * 4096); }
  f32x16 (&sn)[2] = c.S[P ^ 1];
  if constexpr (G == 35) { asm volatile("" : "+v"(sn[0]), "+v"(sn[1])); c.mx0 = fmaxf(sn[0][0], sn[0][1]); c.mx1 = fmaxf(sn[1][0], sn[1][1]); }
  if constexpr (G >= 36 && G < 43) { constexpr int R = 2 + 2 * (G - 36); c.mx0 = fmaxf(fmaxf(c.mx0, sn[0][R]), sn[0][R + 1]); }
  if constexpr (G >= 43 && G < 50) { constexpr int R = 2 + 2 * (G - 43); c.mx1 = fmaxf(fmaxf(c.mx1, sn[1][R]), sn[1][R + 1]); }
  SB0();
}
template <int P, int... Gs> __device__ __forceinline__ void gaps(AttnRegs& c, rsrc_t rK, rsrc_t rV, unsigned ks, unsigned vs, std::integer_sequence<int, Gs...>) {
  (gap<P, Gs>(c, rK, rV, ks, vs), ...);
}
template <int P, int... Fs> __device__ __forceinline__ void frag_reads(AttnRegs& c, std::integer_sequence<int, Fs...>) { (frag_read<P, Fs>(c), ...); }
__device__ __forceinline__ float xhalf_max(float v) {
  auto rr = __builtin_amdgcn_permlane32_swap(__float_as_uint(v), __float_as_uint(v), false, false);
  return fmaxf(__uint_as_float(rr[0]), __uint_as_float(rr[1]));
}
template <bool MERGE, int G> __device__ __forceinline__ void epi1(LDS_AS char* ot, const u32x4 (&sl)[16], int lane, float a1, float a2) {
  f32x4 o1 = acc_read4<4 * G>();
  if constexpr (MERGE) {
    const f32x4 o2 = *reinterpret_cast<const f32x4*>(&sl[G & 15]);
    o1[0] = o1[0] * a1 + o2[0] * a2; o1[1] = o1[1] * a1 + o2[1] * a2; o1[2] = o1[2] * a1 + o2[2] * a2; o1[3] = o1[3] * a1 + o2[3] * a2;
  } else { o1[0] *= a1; o1[1] *= a1; o1[2] *= a1; o1[3] *= a1; }
  u32x2 w = {pk2(o1[0], o1[1]), pk2(o1[2], o1[3])};
  *(LDS_AS u32x2*)(ot + (lane & 31) * 528 + ((G >> 2) * 32 + 8 * (G & 3) + 4 * (lane >> 5)) * 2) = w;
}
template <bool MERGE, int G0, int... Gs> __device__ __forceinline__ void epi_all(LDS_AS char* ot, const u32x4 (&sl)[16], int lane, float a1, float a2, std::integer_sequence<int, Gs...>) {
  (epi1<MERGE, G0 + Gs>(ot, sl, lane, a1, a2), ...);
}
template <int G> __device__ __forceinline__ void slab_store1(rsrc_t r, unsigned voff) {
  const f32x4 v = acc_read4<4 * G>();
  __builtin_amdgcn_raw_buffer_store_b128(*reinterpret_cast<const u32x4*>(&v), r, voff, G * 1024, 16);
}
template <int... Gs> __device__ __forceinline__ void slab_store(rsrc_t r, unsigned voff, std::integer_sequence<int, Gs...>) { (slab_store1<Gs>(r, voff), ...); }

constexpr size_t SLAB_F4 = 4 * (32 * 64 + 64);

__global__ __launch_bounds__(256, 1) void attn_kernel(const u16* __restrict__ Q, const u16* __restrict__ K, const u16* __restrict__ Vt,
                                                      u16* __restrict__ ctx, f32x4* __restrict__ slabs,
                                                      unsigned* __restrict__ cnt,
                                                      const u16* __restrict__ wo, const u16* __restrict__ w2) {
  extern __shared__ __attribute__((aligned(16))) char shm[];
  const int b = blockIdx.x, xcd = b & 7, ii = b >> 3;
  const int h = xcd * 2 + (ii & 1), x = (ii >> 1) & 7, role = ii >> 4;
  const rsrc_t rK = mk_rsrc(K + (size_t)h * SQ * HD, SQ * HD * 2);
  const rsrc_t rV = mk_rsrc(Vt + (size_t)h * HD * SQ, SQ * HD * 2);
  const rsrc_t rQ = mk_rsrc(Q + (size_t)h * SQ * HD, SQ * HD * 2);
  LDS_AS char* lds = (LDS_AS char*)shm;
  const unsigned lds0 = (unsigned)(uintptr_t)lds;
  constexpr int VB0 = 2 * KBUF_B;
  asm volatile("" ::: "a0", "a63", "a127", "a128", "a191", "a255");
  const int nseg = role == 0 ? 1 : 2;

  for (int seg = 0; seg < nseg; ++seg) {
    int tid = threadIdx.x; asm volatile("" : "+v"(tid));
    const int wid = __builtin_amdgcn_readfirstlane(tid >> 6), lane = tid & 63, r32 = lane & 31, hi = lane >> 5;
    const bool partial = seg == 0;
    const int qb = partial ? 15 - x : x;
    const int SPLIT_T = x < 7 ? SPLIT_T_ : 17;
    const int t0 = (partial && role == 1) ? SPLIT_T : 0;
    const int t1 = partial ? (role == 0 ? SPLIT_T : 2 * qb + 2) : 2 * qb + 2;
    const int last = t1 - 1;
    const int q0 = qb * 128 + wid * 32;
    AttnRegs c;
    c.m_run = -1e30f;
    float l_run = 0.f;
    c.kg = (tid >> 5) * 512 + (tid & 31) * 16;
    c.vg = (tid >> 3) * 128 + (tid & 7) * 16;
    c.kw0 = lds0 + (tid >> 5) * 512 + (((tid & 31) ^ (tid >> 5)) << 4);
    c.kw1 = lds0 + ((tid >> 5) + 8) * 512 + (((tid & 31) ^ ((tid >> 5) + 8)) << 4);
    c.vw = lds0 + VB0 + (tid >> 3) * 128 + (((tid & 7) ^ ((tid >> 4) & 7)) << 4);
#pragma unroll
    for (int s = 0; s < 8; ++s) c.kra[s] = lds0 + r32 * 512 + (((2 * s + hi) ^ (r32 & 15)) << 4);
#pragma unroll
    for (int k4 = 0; k4 < 4; ++k4) c.vra[k4] = lds0 + VB0 + r32 * 128 + (((2 * k4 + hi) ^ ((r32 >> 1) & 7)) << 4);
    auto set_mask = [&](int t) {
      const int lim = t > last ? -1000 : q0 + r32 - 64 * t - 4 * hi;
#pragma unroll
      for (int u = 0; u < 2; ++u)
#pragma unroll
        for (int r = 0; r < 16; ++r) c.cin[u][r] = (32 * u + (r & 3) + 8 * (r >> 2)) > lim ? -INFINITY : 0.f;
    };
    auto tclamp = [&](int t) { return (unsigned)(t < last ? t : last) * KBUF_B; };

    __syncthreads();
    stage_load8<0>(rK, c.kg, t0 * KBUF_B, iseq<8>{});
    stage_load8<8>(rV, c.vg, t0 * KBUF_B, iseq<8>{});
    q_load(rQ, r32 * 512 + hi * 16, q0 * 512, iseq<16>{});
    acc_zero(iseq<128>{});
    if (seg == 0) {
      u32x4 wsum = {0u, 0u, 0u, 0u};
#pragma unroll
      for (int i = 0; i < 4; ++i) { const u32x4 v = *reinterpret_cast<const u32x4*>(wo + ((size_t)blockIdx.x * 896 + i * 256 + tid - (i == 3 && tid >= 128 ? 128 : 0)) * 8); wsum ^= v; }
#pragma unroll
      for (int i = 0; i < 2; ++i) { const u32x4 v = *reinterpret_cast<const u32x4*>(w2 + ((size_t)blockIdx.x * 392 + i * 256 + tid - (i == 1 && tid >= 136 ? 136 : 0)) * 8); wsum ^= v; }
      asm volatile("" :: "v"(wsum));
    }
    asm volatile("s_waitcnt vmcnt(16)" ::: "memory");
    stage_write<0>(c.kw0); stage_write<1>(c.kw1); stage_write<2>(c.kw0); stage_write<3>(c.kw1);
    stage_write<4>(c.kw0); stage_write<5>(c.kw1); stage_write<6>(c.kw0); stage_write<7>(c.kw1);
    stage_write<8>(c.vw); stage_write<9>(c.vw); stage_write<10>(c.vw); stage_write<11>(c.vw);
    stage_write<12>(c.vw); stage_write<13>(c.vw); stage_write<14>(c.vw); stage_write<15>(c.vw);
    stage_load8<0>(rK, c.kg, tclamp(t0 + 1), iseq<8>{});
    asm volatile("s_waitcnt vmcnt(0)" ::: "memory");
    { const unsigned a0 = c.kw0 + KBUF_B, a1 = c.kw1 + KBUF_B;
      stage_write<0>(a0); stage_write<1>(a1); stage_write<2>(a0); stage_write<3>(a1);
      stage_write<4>(a0); stage_write<5>(a1); stage_write<6>(a0); stage_write<7>(a1); }
    stage_load8<0>(rK, c.kg, tclamp(t0 + 2), iseq<8>{});
    stage_load8<8>(rV, c.vg, tclamp(t0 + 1), iseq<8>{});
    asm volatile("s_waitcnt lgkmcnt(0)" ::: "memory");
    __builtin_amdgcn_s_barrier();
    set_mask(t0);
#pragma unroll
    for (int u = 0; u < 2; ++u) {
      f32x16 acc = c.cin[u];
#pragma unroll
      for (int s = 0; s < 16; ++s) {
        bf16x8 kf = *(LDS_AS bf16x8*)(uintptr_t)(c.kra[s & 7] + u * 16384 + (s >> 3) * 256);
        switch (s) {
#define QS(n) case n: asm volatile("s_nop 1\n\tv_mfma_f32_32x32x16_bf16 %0, %1, a[%c2:%c3], %0" : "+v"(acc) : "v"(kf), "i"(128 + 4 * n), "i"(131 + 4 * n)); break;
          QS(0) QS(1) QS(2) QS(3) QS(4) QS(5) QS(6) QS(7) QS(8) QS(9) QS(10) QS(11) QS(12) QS(13) QS(14) QS(15)
#undef QS
        }
      }
      asm volatile("s_nop 15\n\ts_nop 15" : "+v"(acc));
      c.S[0][u] = acc;
    }
    {
      float pm = c.S[0][0][0];
#pragma unroll
      for (int r = 1; r < 16; ++r) pm = fmaxf(pm, c.S[0][0][r]);
#pragma unroll
      for (int r = 0; r < 16; ++r) pm = fmaxf(pm, c.S[0][1][r]);
      c.m_run = fmaxf(c.m_run, xhalf_max(pm) * SM_C);
    }
    set_mask(t0 + 1);
    SB0();
    __builtin_amdgcn_s_barrier();
    auto iter = [&](auto par, int t) {
      constexpr int P = decltype(par)::value;
      const unsigned ks = tclamp(t + 3), vs = tclamp(t + 2);
      c.ps = 0.f;
      frag_reads<P>(c, iseq<LEAD>{});
      SB0();
      gaps<P>(c, rK, rV, ks, vs, iseq<64>{});
      l_run += c.ps;
      const float pmax = xhalf_max(fmaxf(c.mx0, c.mx1)) * SM_C;
      if (!__all(pmax - c.m_run <= RESC_THR)) {
        const float mn = fmaxf(c.m_run, pmax);
        const float alpha = __builtin_amdgcn_exp2f(c.m_run - mn);
        c.m_run = mn;
        l_run *= alpha;
        asm volatile("s_nop 15\n\ts_nop 15" ::: "memory");
        acc_scale(alpha, iseq<128>{});
        asm volatile("s_nop 3" ::: "memory");
      }
      if (t + 2 >= 2 * qb || t + 2 > last) set_mask(t + 2);
      SB0();
      asm volatile("s_waitcnt lgkmcnt(0)" ::: "memory");
      __builtin_amdgcn_s_barrier();
    };
    for (int t = t0; t < t1; t += 2) {
      iter(std::integral_constant<int, 0>{}, t);
      if (t + 1 >= t1) break;
      iter(std::integral_constant<int, 1>{}, t + 1);
    }
    asm volatile("s_waitcnt vmcnt(0)\n\ts_nop 15\n\ts_nop 15" ::: "memory");
    {
      auto rr = __builtin_amdgcn_permlane32_swap(__float_as_uint(l_run), __float_as_uint(l_run), false, false);
      l_run = __uint_as_float(rr[0]) + __uint_as_float(rr[1]);
    }
    LDS_AS char* ot = lds + wid * (32 * 528);
    bool do_out = true;
    float a1, a2 = 0.f;
    u32x4 sl[16];
    rsrc_t rot = rK;
    if (partial) {
      const rsrc_t rme = mk_rsrc(slabs + ((size_t)(h * 8 + x) * 2 + role) * SLAB_F4 + wid * (SLAB_F4 / 4), (unsigned)(SLAB_F4 / 4) * 16);
      slab_store(rme, lane * 16, iseq<32>{});
      { const f32x4 ml = {c.m_run, l_run, 0.f, 0.f}; __builtin_amdgcn_raw_buffer_store_b128(*reinterpret_cast<const u32x4*>(&ml), rme, lane * 16, 32 * 1024, 16); }
      asm volatile("s_waitcnt vmcnt(0)" ::: "memory");
      __syncthreads();
      LDS_AS unsigned* flag = (LDS_AS unsigned*)(lds + 4 * 32 * 528);
      if (tid == 0) *flag = __hip_atomic_fetch_add(cnt + h * 8 + x, 1u, __ATOMIC_RELAXED, __HIP_MEMORY_SCOPE_AGENT);
      __syncthreads();
      do_out = (*flag == 1u);
      if (do_out) {
        rot = mk_rsrc(slabs + ((size_t)(h * 8 + x) * 2 + (role ^ 1)) * SLAB_F4 + wid * (SLAB_F4 / 4), (unsigned)(SLAB_F4 / 4) * 16);
        const u32x4 mlu = __builtin_amdgcn_raw_buffer_load_b128(rot, lane * 16, 32 * 1024, 16);
#pragma unroll
        for (int g = 0; g < 16; ++g) sl[g] = __builtin_amdgcn_raw_buffer_load_b128(rot, lane * 16, g * 1024, 16);
        const float m2 = __uint_as_float(mlu[0]), l2 = __uint_as_float(mlu[1]);
        const float mn = fmaxf(c.m_run, m2);
        a1 = __builtin_amdgcn_exp2f(c.m_run - mn); a2 = __builtin_amdgcn_exp2f(m2 - mn);
        const float inv = 1.0f / (l_run * a1 + l2 * a2);
        a1 *= inv; a2 *= inv;
      }
    } else {
      a1 = 1.0f / l_run;
    }
    if (do_out) {
      if (partial) {
        epi_all<true, 0>(ot, sl, lane, a1, a2, iseq<16>{});
        SB0();
#pragma unroll
        for (int g = 0; g < 16; ++g) sl[g] = __builtin_amdgcn_raw_buffer_load_b128(rot, lane * 16, (16 + g) * 1024, 16);
        epi_all<true, 16>(ot, sl, lane, a1, a2, iseq<16>{});
      } else {
        epi_all<false, 0>(ot, sl, lane, a1, a2, iseq<16>{});
        epi_all<false, 16>(ot, sl, lane, a1, a2, iseq<16>{});
      }
      u16* op = ctx + (((size_t)(q0 >> 6) * 64 + h * 4 + ((lane & 31) >> 3)) * 64 + (q0 & 63) + (lane >> 5)) * 64 + (lane & 7) * 8;
#pragma unroll
      for (int it = 0; it < 16; ++it) {
        u32x4 v = *(LDS_AS u32x4*)(ot + (it * 2 + (lane >> 5)) * 528 + (lane & 31) * 16);
        *reinterpret_cast<u32x4*>(op + it * 2 * 64) = v;
      }
    }
  }
}

__device__ __forceinline__ void bdma16(rsrc_t r, unsigned voff, unsigned soff, unsigned lds_dst) {
  asm volatile("s_mov_b32 m0, %0\n\ts_nop 0\n\tbuffer_load_dwordx4 %1, %2, %3 offen lds" :: "s"(lds_dst), "v"(voff), "s"(r), "s"(soff) : "m0", "memory");
}

constexpr int FSTG = 32768, FIN_SHM = 4 * FSTG;
constexpr int FK1 = HDALL / 64, FK2 = FF / 64, FKT = FK1 + FK2;

__global__ __launch_bounds__(512) void final_kernel(const u16* __restrict__ ctx, const u16* __restrict__ wo, const u16* __restrict__ act,
                                                    const u16* __restrict__ w2, const u16* __restrict__ x, u16* __restrict__ out) {
  extern __shared__ __attribute__((aligned(16))) char shm[];
  const int b = blockIdx.x, xi = b >> 3;
  const int pm = (b & 7) * 4 + xi / 7, pn = xi % 7;
  const int brow = pm * 64, bcol = pn * 64, tid = threadIdx.x & 255;
  const int grp = __builtin_amdgcn_readfirstlane(threadIdx.x >> 8);
  const int wid = tid >> 6, lane = tid & 63, wr = wid >> 1, wc = wid & 1, fr = lane & 15, fq = lane >> 4;
  LDS_AS char* lds = (LDS_AS char*)shm + grp * 16384;
  const rsrc_t rC = mk_rsrc(ctx + (size_t)brow * HDALL, 64 * HDALL * 2), rA = mk_rsrc(act + (size_t)brow * FF, 64 * FF * 2);
  const rsrc_t rWo = mk_rsrc(wo, HDALL * MD * 2), rW2 = mk_rsrc(w2, FF * MD * 2);
  const int sr0 = tid >> 3, spc = tid & 7;
  const int acs = spc ^ ((sr0 >> 1) & 7), bcs = spc ^ ((((sr0 >> 1) & 1) | (((sr0 >> 3) & 1) << 1)) << 1);
  const unsigned ac_off = sr0 * 128 + acs * 16, aa_off = sr0 * (FF * 2) + acs * 16;
  const unsigned b_off = sr0 * (MD * 2) + bcol * 2 + bcs * 16;
  const unsigned ldsw = (unsigned)(uintptr_t)lds + __builtin_amdgcn_readfirstlane(wid) * 1024;
  auto dma = [&](int j) {
    const int kt = 2 * j + grp;
    const int kc = kt < FKT ? kt : FKT - 1;
    const unsigned dst = ldsw + (j & 3) * FSTG;
    if (kc < FK1) {
      bdma16(rC, ac_off, kc * 8192, dst); bdma16(rC, ac_off, kc * 8192 + 4096, dst + 4096);
      bdma16(rWo, b_off, kc * 64 * MD * 2, dst + 8192); bdma16(rWo, b_off, (kc * 64 + 32) * MD * 2, dst + 12288);
    } else {
      const int k2 = kc - FK1;
      bdma16(rA, aa_off, k2 * 128, dst); bdma16(rA, aa_off, k2 * 128 + 32 * FF * 2, dst + 4096);
      bdma16(rW2, b_off, k2 * 64 * MD * 2, dst + 8192); bdma16(rW2, b_off, (k2 * 64 + 32) * MD * 2, dst + 12288);
    }
  };
  const int bq = (lane >> 2) & 3, bp = lane & 3;
  int aoff[2], boff[2];
#pragma unroll
  for (int m = 0; m < 2; ++m) { int r = wr * 32 + m * 16 + fr; aoff[m] = r * 128 + ((fq ^ ((r >> 1) & 7)) << 4); }
#pragma unroll
  for (int n = 0; n < 2; ++n) {
    int r = fq * 8 + bq, f = ((r >> 1) & 1) | (((r >> 3) & 1) << 1), cp = wc * 2 + n;
    boff[n] = 8192 + r * 128 + (((cp * 2 + (bp >> 1)) ^ (f << 1)) << 4) + (bp & 1) * 8;
  }
  struct Frag { bf16x8 a[2][2], b[2][2]; };
  auto rdfrag = [&](int slot, Frag& f) {
    LDS_AS char* sb = lds + slot * FSTG;
#pragma unroll
    for (int kk = 0; kk < 2; ++kk) {
#pragma unroll
      for (int m = 0; m < 2; ++m) f.a[kk][m] = *(LDS_AS bf16x8*)(sb + (aoff[m] ^ (kk * 64)));
#pragma unroll
      for (int n = 0; n < 2; ++n) {
        LDS_AS char* p = sb + boff[n] + kk * 4096;
        s16x4 lo = __builtin_amdgcn_ds_read_tr16_b64_v4i16((LDS_AS s16x4*)p);
        s16x4 hh = __builtin_amdgcn_ds_read_tr16_b64_v4i16((LDS_AS s16x4*)(p + 512));
        f.b[kk][n] = (bf16x8){lo[0], lo[1], lo[2], lo[3], hh[0], hh[1], hh[2], hh[3]};
      }
    }
  };
  f32x4 acc[2][2] = {}, accs[2][2];
  auto mma = [&](const Frag& f) {
#pragma unroll
    for (int kk = 0; kk < 2; ++kk)
#pragma unroll
      for (int m = 0; m < 2; ++m)
#pragma unroll
        for (int n = 0; n < 2; ++n) acc[m][n] = __builtin_amdgcn_mfma_f32_16x16x32_bf16(f.b[kk][n], f.a[kk][m], acc[m][n], 0, 0, 0);
  };
  Frag F0, F1;
  dma(0); dma(1); dma(2); dma(3);
  asm volatile("s_waitcnt vmcnt(12)" ::: "memory");
  __builtin_amdgcn_s_barrier();
  rdfrag(0, F0);
#define FSTEP(j, I, FC, FN) do {                                                                                       \
    asm volatile("s_waitcnt vmcnt(8) lgkmcnt(0)" ::: "memory"); __builtin_amdgcn_s_barrier();                         \
    rdfrag(((I) + 1) & 3, FN); dma((j) + 4);                                                                           \
    __builtin_amdgcn_sched_barrier(0); if ((j) < FKT / 2) mma(FC); __builtin_amdgcn_sched_barrier(0); } while (0)
  auto four = [&](int j) { FSTEP(j, 0, F0, F1); FSTEP(j + 1, 1, F1, F0); FSTEP(j + 2, 2, F0, F1); FSTEP(j + 3, 3, F1, F0); };
  for (int kt = 0; kt < FK1 / 2; kt += 4) four(kt);
#pragma unroll
  for (int m = 0; m < 2; ++m)
#pragma unroll
    for (int n = 0; n < 2; ++n) { accs[m][n] = acc[m][n]; acc[m][n] = (f32x4){0.f, 0.f, 0.f, 0.f}; }
  for (int kt = FK1 / 2; kt < FKT / 2; kt += 4) four(kt);
#undef FSTEP
  asm volatile("s_waitcnt vmcnt(0) lgkmcnt(0)" ::: "memory");
  __builtin_amdgcn_s_barrier();
  {
    LDS_AS f32x4* red = (LDS_AS f32x4*)shm;
    if (grp == 1) {
#pragma unroll
      for (int m = 0; m < 2; ++m)
#pragma unroll
        for (int n = 0; n < 2; ++n) { red[(m * 2 + n) * 256 + tid] = accs[m][n]; red[(4 + m * 2 + n) * 256 + tid] = acc[m][n]; }
    }
    __syncthreads();
    if (grp == 1) return;
#pragma unroll
    for (int m = 0; m < 2; ++m)
#pragma unroll
      for (int n = 0; n < 2; ++n) { accs[m][n] += red[(m * 2 + n) * 256 + tid]; acc[m][n] += red[(4 + m * 2 + n) * 256 + tid]; }
  }
#pragma unroll
  for (int m = 0; m < 2; ++m)
#pragma unroll
    for (int n = 0; n < 2; ++n) {
      int row = brow + wr * 32 + m * 16 + fr, col = bcol + wc * 32 + n * 16 + fq * 4;
      s16x4 xv = *reinterpret_cast<const s16x4*>(x + (size_t)row * MD + col);
      float r[4];
#pragma unroll
      for (int j = 0; j < 4; ++j) {
        float s = bfr(accs[m][n][j]);
        float y = bfr(acc[m][n][j]);
        float g = bfr(y / (1.0f + expf(-y)));
        r[j] = bf2f((u16)xv[j]) + bfr(s * g);
      }
      u32x2 w = {pk2(r[0], r[1]), pk2(r[2], r[3])};
      *reinterpret_cast<u32x2*>(out + (size_t)row * MD + col) = w;
    }
}

extern "C" void kernel_launch(void* const* d_in, const int* in_sizes, int n_in, void* d_out, int out_size, void* d_ws, size_t ws_size,
                              hipStream_t stream) {
  const u16* x = (const u16*)d_in[0];
  const u16* ckv = (const u16*)d_in[1];
  const u16* wq = (const u16*)d_in[2];
  const u16* wuk = (const u16*)d_in[3];
  const u16* wuv = (const u16*)d_in[4];
  const u16* wo = (const u16*)d_in[5];
  const u16* w1 = (const u16*)d_in[7];
  const u16* b1 = (const u16*)d_in[8];
  const u16* w2 = (const u16*)d_in[9];
  char* ws = (char*)d_ws;
  u16* xn = (u16*)(ws + WS_XN); u16* Q = (u16*)(ws + WS_Q); u16* K = (u16*)(ws + WS_K); u16* Vt = (u16*)(ws + WS_VT);
  u16* act = (u16*)(ws + WS_ACT); u16* ctx = (u16*)(ws + WS_CTX);
  hipFuncSetAttribute((const void*)proj_gemm_kernel, hipFuncAttributeMaxDynamicSharedMemorySize, GEMM_SHM);
  hipFuncSetAttribute((const void*)attn_kernel, hipFuncAttributeMaxDynamicSharedMemorySize, ATT_SHM);
  ln_kernel<<<SQ / 4, 256, 0, stream>>>(x, xn, (u16*)(ws + WS_LUT), (unsigned*)(ws + WS_CNT));
  proj_gemm_kernel<<<N_GEMM_BLOCKS, 512, GEMM_SHM, stream>>>(ckv, xn, wq, wuk, wuv, w1, b1, Q, K, Vt, act, (unsigned*)(ws + WS_CNT), (const u16*)(ws + WS_LUT));
  attn_kernel<<<256, 256, ATT_SHM, stream>>>(Q, K, Vt, ctx, (f32x4*)(ws + WS_SLAB), (unsigned*)(ws + WS_CNT), wo, w2);
  hipFuncSetAttribute((const void*)final_kernel, hipFuncAttributeMaxDynamicSharedMemorySize, FIN_SHM);
  final_kernel<<<(SQ / 64) * 7, 512, FIN_SHM, stream>>>(ctx, wo, act, w2, x, (u16*)d_out);
}
```
